```python
import torch

def _softmax(x, axis=-1):
    return torch.softmax(x, dim=axis)
def _gelu(x):
    return torch.nn.functional.gelu(x, approximate="tanh")
def _silu(x):
    return torch.nn.functional.silu(x)
def _sigmoid(x):
    return torch.sigmoid(x)
def _layernorm(x, axis=-1, eps=1e-5):
    m = x.mean(dim=axis, keepdim=True)
    v = x.var(dim=axis, keepdim=True, unbiased=False)
    return (x - m) / torch.sqrt(v + eps)
def _scatter_add(tgt, idx, src, axis=0):
    return tgt.clone().index_add_(axis, idx, src)
def _segment_sum(src, seg, n):
    out = torch.zeros((n,) + src.shape[1:], dtype=src.dtype)
    return out.index_add_(0, seg, src)
def _pad(x, pw):
    flat = tuple(v for pair in reversed(pw) for v in pair)
    return torch.nn.functional.pad(x, flat)

kernel_name = 'parallel_sum__grouped_kv_mqa__mixer_channel_mlp__prompt'

def setup_inputs():

    _w = (lambda a: (lambda t: t.to(torch.bfloat16) if t.is_floating_point() else t)(torch.as_tensor(a)))
    def _A_setup():
        B, S, D, H = 1, 2048, 576, 32
        Dh = D // H
        return {
            "x": _w((lambda *s: torch.randn(*s, dtype=torch.bfloat16) * 0.05)(B, S, D)),
            "wq": _w((lambda *s: torch.randn(*s, dtype=torch.bfloat16) * 0.05)(D, H * Dh)),
            "wk": _w((lambda *s: torch.randn(*s, dtype=torch.bfloat16) * 0.05)(D, Dh)),
            "wv": _w((lambda *s: torch.randn(*s, dtype=torch.bfloat16) * 0.05)(D, Dh)),
            "wo": _w((lambda *s: torch.randn(*s, dtype=torch.bfloat16) * 0.05)(H * Dh, D)),
        }
    _A_in = _A_setup()

    def _B_setup():
        B, S, D, F = 1, 2048, 576, 4608
        return {
            "x": _w((lambda *s: torch.randn(*s, dtype=torch.bfloat16) * 0.05)(B, S, D)),
            "w1": _w((lambda *s: torch.randn(*s, dtype=torch.bfloat16) * 3.2)(D, F) * (D ** -0.5)),
            "b1": _w((lambda *s: torch.randn(*s, dtype=torch.bfloat16) * 0.8)(F)),
            "w2": _w((lambda *s: torch.randn(*s, dtype=torch.bfloat16) * 0.1)(F, D) * (F ** -0.5)),
            "b2": _w((lambda *s: torch.randn(*s, dtype=torch.bfloat16) * 0.1)(D)),
        }
    _B_in = _B_setup()

    return {
        "A_x": _A_in["x"],
        "A_wq": _A_in["wq"],
        "A_wk": _A_in["wk"],
        "A_wv": _A_in["wv"],
        "A_wo": _A_in["wo"],
        "B_x": _B_in["x"],
        "B_w1": _B_in["w1"],
        "B_b1": _B_in["b1"],
        "B_w2": _B_in["w2"],
        "B_b2": _B_in["b2"],
    }

def reference(A_x, A_wq, A_wk, A_wv, A_wo, B_x, B_w1, B_b1, B_w2, B_b2):
    def _A_ref(x, wq, wk, wv, wo):
        B, S, D = x.shape
        H = 32
        Dh = D // H
        q = (lambda x, *d: x.permute(*d))((x @ wq).reshape(B, S, H, Dh), 0, 2, 1, 3)
        k = x @ wk
        v = x @ wv
        scores = torch.einsum("bhqd,bkd->bhqk", q, k) * (Dh ** -0.5)
        p = _softmax(scores, axis=-1)
        o = torch.einsum("bhqk,bkd->bhqd", p, v)
        return (lambda x, *d: x.permute(*d))(o, 0, 2, 1, 3).reshape(B, S, H * Dh) @ wo

    def _B_ref(x, w1, b1, w2, b2):
        y = _layernorm(x, axis=-1)
        h = _gelu(torch.einsum("bsd,df->bsf", y, w1) + b1)
        return torch.einsum("bsf,fd->bsd", h, w2) + b2

    x = A_x
    xn = _layernorm(x, axis=-1)
    return x + _A_ref(xn, A_wq, A_wk, A_wv, A_wo) + _B_ref(xn, B_w1, B_b1, B_w2, B_b2)
```

```cpp
#include <hip/hip_runtime.h>
#include <hip/hip_bf16.h>
#include <stdint.h>

typedef short bf16x8 __attribute__((ext_vector_type(8)));
typedef short bf16x4 __attribute__((ext_vector_type(4)));
typedef __bf16 bfx2 __attribute__((ext_vector_type(2)));
typedef float f32x2 __attribute__((ext_vector_type(2)));
typedef float f32x4 __attribute__((ext_vector_type(4)));
typedef float f32x16 __attribute__((ext_vector_type(16)));
typedef unsigned u32x2 __attribute__((ext_vector_type(2)));
typedef unsigned u32x4 __attribute__((ext_vector_type(4)));
typedef unsigned short u16;
#define LDSP __attribute__((address_space(3)))
#define DI __device__ __forceinline__
#define MFMA32(a, b, c) __builtin_amdgcn_mfma_f32_32x32x16_bf16((a), (b), (c), 0, 0, 0)

constexpr int SEQ = 2048, DM = 576, FF = 4608;
constexpr float LN_EPS = 1e-5f;
constexpr size_t WS_HF = 0;
constexpr size_t WS_Q = WS_HF + (size_t)64 * 288 * 1024;
constexpr size_t WS_KV = WS_Q + (size_t)SEQ * DM * 2;
constexpr size_t WS_OF = WS_KV + (size_t)64 * 2560;
constexpr size_t WS_PART = WS_OF + (size_t)64 * 36 * 1024;

DI unsigned pack2(float a, float b) {
  f32x2 p = {a, b};
  return __builtin_bit_cast(unsigned, __builtin_convertvector(p, bfx2));
}
DI float dot2(unsigned a, unsigned b, float c) {
  return __builtin_amdgcn_fdot2_f32_bf16(__builtin_bit_cast(bfx2, a), __builtin_bit_cast(bfx2, b), c, false);
}
DI float bf2f(u16 v) { return __uint_as_float((unsigned)v << 16); }
DI void glds16(const void* g, char* lds) {
  __builtin_amdgcn_global_load_lds(g, (LDSP void*)lds, 16, 0, 0);
}
DI bf16x8 tr_frag(const char* lds_a, const char* lds_b) {
  bf16x4 a = __builtin_amdgcn_ds_read_tr16_b64_v4i16((LDSP bf16x4*)lds_a);
  bf16x4 b = __builtin_amdgcn_ds_read_tr16_b64_v4i16((LDSP bf16x4*)lds_b);
  return __builtin_shufflevector(a, b, 0, 1, 2, 3, 4, 5, 6, 7);
}
DI bf16x8 lds_frag(const char* p) { return *(const bf16x8*)p; }
DI float xhalf_sum(float v) { return v + __shfl_xor(v, 32); }
#define WAIT_VM0() asm volatile("s_waitcnt vmcnt(0)" ::: "memory")
#define BARRIER() __builtin_amdgcn_s_barrier()
#define ONES2 0x3f803f80u

DI void frag_stats(const bf16x8& f, float& sx, float& sxx) {
  u32x4 u = __builtin_bit_cast(u32x4, f);
#pragma unroll
  for (int i = 0; i < 4; ++i) { sx = dot2(u[i], ONES2, sx); sxx = dot2(u[i], u[i], sxx); }
}
DI void frag_sum(const bf16x8& f, float& s) {
  u32x4 u = __builtin_bit_cast(u32x4, f);
#pragma unroll
  for (int i = 0; i < 4; ++i) s = dot2(u[i], ONES2, s);
}
DI void ln_affine(float sx, float sxx, float& a1, float& c1, float& a2, float& c2) {
  const float m = sx * (1.0f / DM);
  float v = sxx * (1.0f / DM) - m * m;
  v = fmaxf(v, 0.0f);
  const float r1 = 1.0f / sqrtf(v + LN_EPS);
  const float v2 = v * r1 * r1;
  const float r2 = 1.0f / sqrtf(v2 + LN_EPS);
  a1 = r1; c1 = -m * r1;
  a2 = r1 * r2; c2 = -m * a2;
}
DI float gelu_tanh(float x) {
  const float k0 = -2.0f * 0.7978845608028654f * 1.4426950408889634f;
  const float k1 = k0 * 0.044715f;
  const float z = x * fmaf(x * x, k1, k0);
  const float e = __builtin_amdgcn_exp2f(z);
  return x * __builtin_amdgcn_rcpf(1.0f + e);
}

constexpr int KA_STAGE = 57344;
constexpr int KA_LDS = 148480;

DI void ka_gemm_tile(char* smem, const u16* __restrict__ x, const char* __restrict__ wsrc, int ldw,
                     const u16* __restrict__ bias, bool isq, int rb, int cb, char* __restrict__ ws) {
  const int tid = threadIdx.x, l = tid & 63;
  const int w = __builtin_amdgcn_readfirstlane(tid >> 6);
  const int wm = w & 1, wn = w >> 1;
  const int r = l & 31, h = l >> 5;
  unsigned woff[3], xoff[4];
#pragma unroll
  for (int i = 0; i < 3; ++i) {
    const int p = 3 * w + i, c = p >> 3, rg = p & 7;
    const int row = rg * 8 + (l >> 3), ch = (l & 7) ^ (((l >> 4) & 1) << 2);
    woff[i] = row * ldw + c * 128 + ch * 16;
  }
#pragma unroll
  for (int i = 0; i < 4; ++i) {
    const int tg = 4 * w + i, row = tg * 8 + (l >> 3), ch = (l & 7) ^ ((row >> 1) & 7);
    xoff[i] = row * 1152 + ch * 16;
  }
  const char* xsrc = (const char*)x + (size_t)rb * 256 * 1152;
  auto stage = [&](int buf, int kt) {
    char* sb = smem + buf * KA_STAGE;
    const char* wb = wsrc + (size_t)kt * 64 * ldw;
    const char* xb = xsrc + kt * 128;
#pragma unroll
    for (int i = 0; i < 3; ++i) glds16(wb + woff[i], sb + (3 * w + i) * 1024);
#pragma unroll
    for (int i = 0; i < 4; ++i) glds16(xb + xoff[i], sb + 24576 + (4 * w + i) * 1024);
  };
  int aoff[3], boff[4];
  {
    const int g = l >> 4, q = (l & 15) >> 2, p = l & 3;
    const int abase = (8 * (g >> 1) + q) * 128 + (g & 1) * 32 + p * 8, swz = ((q >> 1) & 1) * 64;
#pragma unroll
    for (int m = 0; m < 3; ++m) {
      const int mt = wm * 3 + m;
      aoff[m] = (mt >> 1) * 8192 + abase + (((mt & 1) * 64) ^ swz);
    }
#pragma unroll
    for (int ks = 0; ks < 4; ++ks) boff[ks] = 24576 + wn * 8192 + r * 128 + (((2 * ks + h) ^ ((r >> 1) & 7)) << 4);
  }
  const int n0off = wm * 4096, n1off = (wm ^ 1) * 4096;
  f32x16 acc[3][2];
#pragma unroll
  for (int m = 0; m < 3; ++m)
#pragma unroll
    for (int n = 0; n < 2; ++n)
#pragma unroll
      for (int i = 0; i < 16; ++i) acc[m][n][i] = 0.f;
  float sx = 0.f, sxx = 0.f, cs[3] = {0.f, 0.f, 0.f};

  stage(0, 0);
#pragma unroll 1
  for (int kt = 0; kt < 9; ++kt) {
    WAIT_VM0();
    BARRIER();
    if (kt + 1 < 9) stage((kt + 1) & 1, kt + 1);
    const char* sb = smem + (kt & 1) * KA_STAGE;
#pragma unroll
    for (int ks = 0; ks < 4; ++ks) {
      bf16x8 a[3], b[2];
#pragma unroll
      for (int m = 0; m < 3; ++m) a[m] = tr_frag(sb + aoff[m] + ks * 2048, sb + aoff[m] + ks * 2048 + 512);
      b[0] = lds_frag(sb + boff[ks] + n0off);
      b[1] = lds_frag(sb + boff[ks] + n1off);
#pragma unroll
      for (int m = 0; m < 3; ++m)
#pragma unroll
        for (int n = 0; n < 2; ++n) acc[m][n] = MFMA32(a[m], b[n], acc[m][n]);
      frag_stats(b[0], sx, sxx);
      if (ks == wn) {
#pragma unroll
        for (int m = 0; m < 3; ++m) frag_sum(a[m], cs[m]);
      }
    }
  }
  __syncthreads();
  float* csp = (float*)smem;
  float* csf = (float*)(smem + 6144);
  float* bsf = (float*)(smem + 6912);
  f32x2* st = (f32x2*)(smem + 7680);
#pragma unroll
  for (int m = 0; m < 3; ++m) csp[(wn * 2 + h) * 192 + (wm * 3 + m) * 32 + r] = cs[m];
  float av[2], cv[2];
  {
    sx = xhalf_sum(sx); sxx = xhalf_sum(sxx);
    float a1, c1, a2, c2;
    ln_affine(sx, sxx, a1, c1, a2, c2);
    const float qs = 1.4426950408889634f * 0.23570226039551584f;
    av[0] = isq ? a1 * qs : a2;
    cv[0] = isq ? c1 * qs : c2;
    if (h == 0) st[wn * 64 + wm * 32 + r] = (f32x2){av[0], cv[0]};
  }
  __syncthreads();
  if (tid < 192) {
    float s = 0.f;
#pragma unroll
    for (int i = 0; i < 8; ++i) s += csp[i * 192 + tid];
    csf[tid] = s;
    bsf[tid] = isq ? 0.f : bf2f(bias[cb * 192 + tid]);
  }
  {
    const f32x2 o = st[wn * 64 + (wm ^ 1) * 32 + r];
    av[1] = o[0]; cv[1] = o[1];
  }
  __syncthreads();
#pragma unroll
  for (int m = 0; m < 3; ++m) {
    const int mt = wm * 3 + m;
    f32x4 c4[4], b4[4];
#pragma unroll
    for (int g = 0; g < 4; ++g) {
      c4[g] = *(const f32x4*)&csf[mt * 32 + 8 * g + 4 * h];
      b4[g] = *(const f32x4*)&bsf[mt * 32 + 8 * g + 4 * h];
    }
#pragma unroll
    for (int n = 0; n < 2; ++n) {
      const int nt = wn * 2 + (n ^ wm);
      float v[16];
#pragma unroll
      for (int i = 0; i < 16; ++i) v[i] = fmaf(av[n], acc[m][n][i], fmaf(cv[n], c4[i >> 2][i & 3], b4[i >> 2][i & 3]));
      if (!isq) {
#pragma unroll
        for (int i = 0; i < 16; ++i) v[i] = gelu_tanh(v[i]);
        char* dst = ws + WS_HF + ((size_t)(rb * 8 + nt) * 288 + cb * 12 + mt * 2) * 1024 + l * 16;
#pragma unroll
        for (int s = 0; s < 2; ++s) {
          u32x4 u = {pack2(v[8 * s], v[8 * s + 1]), pack2(v[8 * s + 2], v[8 * s + 3]),
                     pack2(v[8 * s + 4], v[8 * s + 5]), pack2(v[8 * s + 6], v[8 * s + 7])};
          *(u32x4*)(dst + s * 1024) = u;
        }
      } else {
        char* dst = ws + WS_Q + ((size_t)(rb * 256 + nt * 32 + r) * DM + cb * 192 + mt * 32 + 4 * h) * 2;
#pragma unroll
        for (int g = 0; g < 4; ++g) {
          u32x2 u = {pack2(v[4 * g], v[4 * g + 1]), pack2(v[4 * g + 2], v[4 * g + 3])};
          *(u32x2*)(dst + g * 16) = u;
        }
      }
    }
  }
}

DI void ka_kv_tile(char* smem, const u16* __restrict__ x, const u16* __restrict__ wk, const u16* __restrict__ wv,
                   int tb, char* __restrict__ ws) {
  const int tid = threadIdx.x, l = tid & 63;
  const int w = __builtin_amdgcn_readfirstlane(tid >> 6);
  const int r = l & 31, h = l >> 5;
  char* ximg = smem + 73728;
  {
    const int row = w * 8 + (l >> 3), ch = (l & 7) ^ ((row >> 1) & 7);
    const char* src = (const char*)x + (size_t)(tb * 64 + row) * 1152 + ch * 16;
#pragma unroll
    for (int kt = 0; kt < 9; ++kt) glds16(src + kt * 128, ximg + kt * 8192 + w * 1024);
  }
  for (int idx = tid; idx < 2 * 5184; idx += 512) {
    const int mat = idx >= 5184, rem = idx - mat * 5184, row = rem / 9, dw = rem - row * 9;
    const unsigned v = ((const unsigned*)(mat ? wv : wk))[rem];
    *(unsigned*)(smem + row * 128 + ((mat ^ ((row >> 1) & 1)) * 64) + dw * 4) = v;
  }
  WAIT_VM0();
  __syncthreads();
  if (w >= 4) return;
  const int mat = w >> 1, t = w & 1;
  const int g = l >> 4, q = (l & 15) >> 2, p = l & 3;
  const int aoff = (8 * (g >> 1) + q) * 128 + (g & 1) * 32 + p * 8 + ((mat * 64) ^ (((q >> 1) & 1) * 64));
  int boff[4];
#pragma unroll
  for (int ks = 0; ks < 4; ++ks) boff[ks] = t * 4096 + r * 128 + (((2 * ks + h) ^ ((r >> 1) & 7)) << 4);
  f32x16 acc;
#pragma unroll
  for (int i = 0; i < 16; ++i) acc[i] = 0.f;
  float sx = 0.f, sxx = 0.f, cs = 0.f;
#pragma unroll 1
  for (int kt = 0; kt < 9; ++kt) {
#pragma unroll
    for (int ks = 0; ks < 4; ++ks) {
      const char* ap = smem + kt * 8192 + ks * 2048 + aoff;
      const bf16x8 wf = tr_frag(ap, ap + 512);
      const bf16x8 xf = lds_frag(ximg + kt * 8192 + boff[ks]);
      if (mat == 0) acc = MFMA32(wf, xf, acc); else acc = MFMA32(xf, wf, acc);
      frag_stats(xf, sx, sxx);
      frag_sum(wf, cs);
    }
  }
  sx = xhalf_sum(sx); sxx = xhalf_sum(sxx); cs = xhalf_sum(cs);
  float a1, c1, a2, c2;
  ln_affine(sx, sxx, a1, c1, a2, c2);
  float* scr = (float*)(smem + 147456 + w * 256);
  char* img = ws + WS_KV + (size_t)(tb * 2 + t) * 2560;
  if (mat == 0) {
    if (h == 0) scr[r] = cs;
    __builtin_amdgcn_s_waitcnt(0xc07f);
    __builtin_amdgcn_wave_barrier();
    float v[12];
#pragma unroll
    for (int gg = 0; gg < 3; ++gg) {
      const f32x4 c4 = *(const f32x4*)&scr[8 * gg + 4 * h];
#pragma unroll
      for (int e = 0; e < 4; ++e) v[4 * gg + e] = fmaf(a1, acc[4 * gg + e], c1 * c4[e]);
    }
    u32x4 u = {pack2(v[0], v[1]), pack2(v[2], v[3]), pack2(v[4], v[5]), pack2(v[6], v[7])};
    *(u32x4*)(img + l * 16) = u;
    if (h == 0) {
      u32x2 u2 = {pack2(v[8], v[9]), 0x00003f80u};
      *(u32x2*)(img + 1024 + r * 8) = u2;
    }
  } else {
    if (h == 0) { scr[r] = a1; scr[32 + r] = c1; }
    __builtin_amdgcn_s_waitcnt(0xc07f);
    __builtin_amdgcn_wave_barrier();
    float v[16];
#pragma unroll
    for (int gg = 0; gg < 4; ++gg) {
      const f32x4 a4 = *(const f32x4*)&scr[8 * gg + 4 * h];
      const f32x4 c4 = *(const f32x4*)&scr[32 + 8 * gg + 4 * h];
#pragma unroll
      for (int e = 0; e < 4; ++e) v[4 * gg + e] = fmaf(a4[e], acc[4 * gg + e], c4[e] * cs);
    }
    if (r < 20) {
#pragma unroll
      for (int s = 0; s < 2; ++s) {
        u32x4 u = {pack2(v[8 * s], v[8 * s + 1]), pack2(v[8 * s + 2], v[8 * s + 3]),
                   pack2(v[8 * s + 4], v[8 * s + 5]), pack2(v[8 * s + 6], v[8 * s + 7])};
        if (r == 18) u = (u32x4){ONES2, ONES2, ONES2, ONES2};
        if (r == 19) u = (u32x4){0u, 0u, 0u, 0u};
        *(u32x4*)(img + 1280 + ((s * 2 + h) * 20 + r) * 16) = u;
      }
    }
  }
}

__global__ __launch_bounds__(512, 2) void ka_kernel(const u16* __restrict__ x, const u16* __restrict__ wq,
                                                    const u16* __restrict__ wk, const u16* __restrict__ wv,
                                                    const u16* __restrict__ w1, const u16* __restrict__ b1,
                                                    char* __restrict__ ws) {
  extern __shared__ __attribute__((aligned(16))) char smem[];
  const int b = blockIdx.x, xcd = b & 7, j = b >> 3;
  const int rh = xcd >> 2, sq = xcd & 3;
  if (j < 27) {
    const bool isq = j >= 24;
    const int i = sq * 3 + (j - 24);
    const int rb = rh * 4 + (isq ? (i & 3) : (j & 3)), cb = isq ? (i >> 2) : sq * 6 + (j >> 2);
    ka_gemm_tile(smem, x, (const char*)(isq ? wq : w1) + cb * 384, isq ? DM * 2 : FF * 2, b1, isq, rb, cb, ws);
  } else {
    ka_kv_tile(smem, x, wk, wv, rh * 16 + sq * 4 + (j - 27), ws);
  }
}

constexpr int KB_LDS = 73728 + 64;

template <bool SAFE>
DI void attn_loop(char* smem, const char* __restrict__ kvimg, int w, int l, const bf16x8 (&q0)[2], u32x4 (&q1)[2],
                  f32x16 (&o)[2]) {
  const int r = l & 31, h = l >> 5;
  const int dv = (r == 22) ? 18 : (r >= 20 ? r - 16 : r);
  const int koff = l * 16, k1off = 1024 + r * 8, voff = 1280 + (h * 20 + dv) * 16;
  auto stage = [&](int c) {
#pragma unroll
    for (int i = 0; i < 5; ++i)
      glds16(kvimg + (size_t)c * 20480 + (5 * w + i) * 1024 + l * 16, smem + (c & 1) * 20480 + (5 * w + i) * 1024);
  };
  float mrun[2] = {-3.0e38f, -3.0e38f};
#pragma unroll
  for (int qt = 0; qt < 2; ++qt)
#pragma unroll
    for (int i = 0; i < 16; ++i) o[qt][i] = 0.f;
  const f32x16 zero = {};
  stage(0);
#pragma unroll 1
  for (int c = 0; c < 8; ++c) {
    WAIT_VM0();
    BARRIER();
    if (c + 1 < 8) stage(c + 1);
    const char* sb = smem + (c & 1) * 20480;
    if (!SAFE && c == 0) {
      const bf16x8 kf0 = lds_frag(sb + koff);
      const u32x2 k1 = *(const u32x2*)(sb + k1off);
      const u32x4 k1v = {k1[0], k1[1], 0u, 0u};
#pragma unroll
      for (int qt = 0; qt < 2; ++qt) {
        f32x16 s = MFMA32(kf0, q0[qt], zero);
        s = MFMA32(__builtin_bit_cast(bf16x8, k1v), __builtin_bit_cast(bf16x8, q1[qt]), s);
        float m = s[0];
#pragma unroll
        for (int i = 1; i < 16; ++i) m = fmaxf(m, s[i]);
        m = fmaxf(m, __shfl_xor(m, 32));
        if (h == 0) q1[qt][1] = pack2(-m, 0.f);
      }
    }
#pragma unroll 2
    for (int t = 0; t < 8; ++t) {
      const char* tb = sb + t * 2560;
      const bf16x8 kf0 = lds_frag(tb + koff);
      const u32x2 k1 = *(const u32x2*)(tb + k1off);
      const u32x4 k1v = {k1[0], k1[1], 0u, 0u};
      const bf16x8 vf0 = lds_frag(tb + voff), vf1 = lds_frag(tb + voff + 640);
#pragma unroll
      for (int qt = 0; qt < 2; ++qt) {
        f32x16 s = MFMA32(kf0, q0[qt], zero);
        s = MFMA32(__builtin_bit_cast(bf16x8, k1v), __builtin_bit_cast(bf16x8, q1[qt]), s);
        if (SAFE) {
          float m = s[0];
#pragma unroll
          for (int i = 1; i < 16; ++i) m = fmaxf(m, s[i]);
          m = fmaxf(m, __shfl_xor(m, 32));
          const float mn = fmaxf(mrun[qt], m);
          const float alpha = __builtin_amdgcn_exp2f(mrun[qt] - mn);
          mrun[qt] = mn;
#pragma unroll
          for (int i = 0; i < 16; ++i) { o[qt][i] *= alpha; s[i] -= mn; }
        }
        unsigned p[8];
#pragma unroll
        for (int i = 0; i < 8; ++i)
          p[i] = pack2(__builtin_amdgcn_exp2f(s[2 * i]), __builtin_amdgcn_exp2f(s[2 * i + 1]));
        const u32x4 pa = {p[0], p[1], p[2], p[3]}, pb = {p[4], p[5], p[6], p[7]};
        o[qt] = MFMA32(vf0, __builtin_bit_cast(bf16x8, pa), o[qt]);
        o[qt] = MFMA32(vf1, __builtin_bit_cast(bf16x8, pb), o[qt]);
      }
    }
  }
}

DI void kb_attn(char* smem, int id, char* __restrict__ ws) {
  const int tid = threadIdx.x, l = tid & 63;
  const int w = __builtin_amdgcn_readfirstlane(tid >> 6);
  const int r = l & 31, h = l >> 5;
  const int head = id & 31, tblk = id >> 5;
  const int tt0 = tblk * 8 + w * 2;
  bf16x8 q0[2];
  u32x4 q1[2];
#pragma unroll
  for (int qt = 0; qt < 2; ++qt) {
    const unsigned* qp = (const unsigned*)(ws + WS_Q + ((size_t)((tt0 + qt) * 32 + r) * DM + head * 18) * 2);
    u32x4 u = {qp[2 * h], qp[2 * h + 1], qp[4 + 2 * h], qp[5 + 2 * h]};
    q0[qt] = __builtin_bit_cast(bf16x8, u);
    const unsigned e = qp[8];
    q1[qt] = (u32x4){h == 0 ? e : 0u, 0u, 0u, 0u};
  }
  f32x16 o[2];
  const char* kvimg = ws + WS_KV;
  attn_loop<false>(smem, kvimg, w, l, q0, q1, o);
  bool bad = false;
#pragma unroll
  for (int qt = 0; qt < 2; ++qt) {
    bad |= !(o[qt][10] <= 1.8446744e19f);
#pragma unroll
    for (int i = 0; i < 10; ++i) bad |= !(fabsf(o[qt][i]) <= 3.0e38f);
  }
  volatile unsigned* flag = (volatile unsigned*)(smem + 73728);
  if (tid == 0) *flag = 0u;
  __syncthreads();
  if (bad) *flag = 1u;
  __syncthreads();
  if (__builtin_amdgcn_readfirstlane(*flag) != 0u) {
    q1[0][1] = 0u; q1[1][1] = 0u;
    attn_loop<true>(smem, kvimg, w, l, q0, q1, o);
  }
#pragma unroll
  for (int qt = 0; qt < 2; ++qt) {
    const float inv = 1.0f / o[qt][10];
    char* dst = ws + WS_OF + (size_t)(tt0 + qt) * 36 * 1024;
    u32x4 u = {pack2(o[qt][0] * inv, o[qt][1] * inv), pack2(o[qt][2] * inv, o[qt][3] * inv),
               pack2(o[qt][4] * inv, o[qt][5] * inv), pack2(o[qt][6] * inv, o[qt][7] * inv)};
    *(u32x4*)(dst + head * 1024 + l * 16) = u;
    if (h == 0)
      *(unsigned*)(dst + (32 + (head >> 3)) * 1024 + (((head >> 2) & 1) * 32 + r) * 16 + (head & 3) * 4) =
          pack2(o[qt][8] * inv, o[qt][9] * inv);
  }
}

DI void kb_gemm2(char* smem, int id, const u16* __restrict__ w2, char* __restrict__ ws) {
  const int tid = threadIdx.x, l = tid & 63;
  const int w = __builtin_amdgcn_readfirstlane(tid >> 6);
  const int xcd = id & 7, j = id >> 3;
  const int s = xcd & 3, tb = (xcd >> 2) * 16 + (j >> 1), nh = j & 1;
  const int tt = w & 1, kk = w >> 1;
  const int h = l >> 5;
  unsigned woff[9];
#pragma unroll
  for (int i = 0; i < 9; ++i) {
    const int idx = (9 * w + i) * 64 + l, row = idx / 36, c16 = idx - row * 36;
    woff[i] = row * 1152 + c16 * 16;
  }
  const char* wsrc = (const char*)w2 + (size_t)s * 1152 * 1152 + nh * 576;
  const char* hsrc = ws + WS_HF + ((size_t)(tb * 2 + tt) * 288 + s * 72 + kk * 2) * 1024 + l * 16;
  auto stage = [&](int kt) {
    const char* wb = wsrc + (size_t)kt * 64 * 1152;
    char* sb = smem + (kt & 1) * 36864;
#pragma unroll
    for (int i = 0; i < 9; ++i) glds16(wb + woff[i], sb + (9 * w + i) * 1024);
  };
  const int g = l >> 4, q = (l & 15) >> 2, p = l & 3;
  const int aoff = (kk * 32 + 4 * h + q) * 576 + (g & 1) * 32 + p * 8;
  f32x16 acc[9];
#pragma unroll
  for (int m = 0; m < 9; ++m)
#pragma unroll
    for (int i = 0; i < 16; ++i) acc[m][i] = 0.f;
  bf16x8 hn[2];
  stage(0);
  hn[0] = *(const bf16x8*)(hsrc);
  hn[1] = *(const bf16x8*)(hsrc + 1024);
#pragma unroll 1
  for (int kt = 0; kt < 18; ++kt) {
    WAIT_VM0();
    asm volatile("" : "+v"(hn[0]), "+v"(hn[1]));
    BARRIER();
    const bf16x8 hc0 = hn[0], hc1 = hn[1];
    if (kt + 1 < 18) {
      stage(kt + 1);
      hn[0] = *(const bf16x8*)(hsrc + (kt + 1) * 4096);
      hn[1] = *(const bf16x8*)(hsrc + (kt + 1) * 4096 + 1024);
    }
    const char* sb = smem + (kt & 1) * 36864 + aoff;
#pragma unroll
    for (int m = 0; m < 9; ++m) {
      const bf16x8 a = tr_frag(sb + m * 64, sb + m * 64 + 8 * 576);
      acc[m] = MFMA32(a, hc0, acc[m]);
    }
#pragma unroll
    for (int m = 0; m < 9; ++m) {
      const bf16x8 a = tr_frag(sb + 16 * 576 + m * 64, sb + 16 * 576 + m * 64 + 8 * 576);
      acc[m] = MFMA32(a, hc1, acc[m]);
    }
  }
  __syncthreads();
  char* red = smem + tt * 36864 + l * 16;
  if (kk == 1) {
#pragma unroll
    for (int m = 0; m < 9; ++m)
#pragma unroll
      for (int gq = 0; gq < 4; ++gq)
        *(f32x4*)(red + (m * 4 + gq) * 1024) = (f32x4){acc[m][4 * gq], acc[m][4 * gq + 1], acc[m][4 * gq + 2], acc[m][4 * gq + 3]};
  }
  __syncthreads();
  if (kk == 0) {
#pragma unroll
    for (int m = 0; m < 9; ++m) {
      char* dst = ws + WS_PART + (((size_t)s * 18 + nh * 9 + m) * 64 + tb * 2 + tt) * 4096 + l * 16;
#pragma unroll
      for (int gq = 0; gq < 4; ++gq) {
        const f32x4 v = *(const f32x4*)(red + (m * 4 + gq) * 1024);
        *(f32x4*)(dst + gq * 1024) = (f32x4){acc[m][4 * gq] + v[0], acc[m][4 * gq + 1] + v[1], acc[m][4 * gq + 2] + v[2],
                                             acc[m][4 * gq + 3] + v[3]};
      }
    }
  }
}

__global__ __launch_bounds__(256, 2) void kb_kernel(const u16* __restrict__ w2, char* __restrict__ ws) {
  extern __shared__ __attribute__((aligned(16))) char smem[];
  const int b = blockIdx.x;
  if (b < 256) kb_attn(smem, b, ws);
  else kb_gemm2(smem, b - 256, w2, ws);
}

constexpr int KC_LDS = 73728;

__global__ __launch_bounds__(256, 2) void kc_kernel(const u16* __restrict__ x, const u16* __restrict__ wo,
                                                    const u16* __restrict__ b2, const char* __restrict__ ws,
                                                    u16* __restrict__ out) {
  extern __shared__ __attribute__((aligned(16))) char smem[];
  const int tid = threadIdx.x, l = tid & 63;
  const int w = __builtin_amdgcn_readfirstlane(tid >> 6);
  const int nb = blockIdx.x % 9, tb = blockIdx.x / 9;
  const int wm = w & 1, wn = w >> 1;
  const int r = l & 31, h = l >> 5;
#pragma unroll
  for (int i = 0; i < 18; ++i) {
    const int pc = 18 * w + i, kp = pc * 8 + (l >> 3);
    int row;
    if (kp < 512) row = (kp >> 4) * 18 + (kp & 15);
    else {
      const int rr = (kp - 512) >> 4, ii = kp & 15, hh = (ii >> 2) & 1, jj = 4 * (ii >> 3) + (ii & 3);
      row = 18 * (8 * rr + 4 * hh + (jj >> 1)) + 16 + (jj & 1);
    }
    const int ch = (l & 7) ^ (((kp >> 1) & 1) << 2);
    glds16((const char*)wo + (size_t)row * 1152 + nb * 128 + ch * 16, smem + pc * 1024);
  }
  const int ttile = tb * 2 + wn, ntile = nb * 2 + wm;
  f32x16 acc;
#pragma unroll
  for (int i = 0; i < 16; ++i) acc[i] = 0.f;
#pragma unroll
  for (int s = 0; s < 4; ++s) {
    const char* src = ws + WS_PART + (((size_t)s * 18 + ntile) * 64 + ttile) * 4096 + l * 16;
#pragma unroll
    for (int gq = 0; gq < 4; ++gq) {
      const f32x4 v = *(const f32x4*)(src + gq * 1024);
#pragma unroll
      for (int e = 0; e < 4; ++e) acc[4 * gq + e] += v[e];
    }
  }
  const char* osrc = ws + WS_OF + (size_t)ttile * 36 * 1024 + l * 16;
  bf16x8 of[36];
#pragma unroll
  for (int ks = 0; ks < 36; ++ks) of[ks] = *(const bf16x8*)(osrc + ks * 1024);
  WAIT_VM0();
  __syncthreads();
  const int g = l >> 4, q = (l & 15) >> 2, p = l & 3;
  const int aoff = (4 * h + q) * 128 + (g & 1) * 32 + p * 8 + ((wm * 64) ^ (((q >> 1) & 1) * 64));
#pragma unroll
  for (int ks = 0; ks < 36; ++ks) {
    const bf16x8 a = tr_frag(smem + aoff + ks * 2048, smem + aoff + ks * 2048 + 1024);
    acc = MFMA32(a, of[ks], acc);
  }
  const size_t obase = (size_t)(ttile * 32 + r) * DM + ntile * 32 + 4 * h;
#pragma unroll
  for (int gq = 0; gq < 4; ++gq) {
    const u32x2 xv = *(const u32x2*)(x + obase + 8 * gq);
    const u32x2 bv = *(const u32x2*)(b2 + ntile * 32 + 4 * h + 8 * gq);
    float v[4];
    v[0] = acc[4 * gq + 0] + __uint_as_float(xv[0] << 16) + __uint_as_float(bv[0] << 16);
    v[1] = acc[4 * gq + 1] + __uint_as_float(xv[0] & 0xffff0000u) + __uint_as_float(bv[0] & 0xffff0000u);
    v[2] = acc[4 * gq + 2] + __uint_as_float(xv[1] << 16) + __uint_as_float(bv[1] << 16);
    v[3] = acc[4 * gq + 3] + __uint_as_float(xv[1] & 0xffff0000u) + __uint_as_float(bv[1] & 0xffff0000u);
    u32x2 u = {pack2(v[0], v[1]), pack2(v[2], v[3])};
    *(u32x2*)(out + obase + 8 * gq) = u;
  }
}

extern "C" void kernel_launch(void* const* d_in, const int* in_sizes, int n_in, void* d_out, int out_size,
                              void* d_ws, size_t ws_size, hipStream_t stream) {
  const u16* x = (const u16*)d_in[0];
  const u16* wq = (const u16*)d_in[1];
  const u16* wk = (const u16*)d_in[2];
  const u16* wv = (const u16*)d_in[3];
  const u16* wo = (const u16*)d_in[4];
  const u16* w1 = (const u16*)d_in[6];
  const u16* b1 = (const u16*)d_in[7];
  const u16* w2 = (const u16*)d_in[8];
  const u16* b2 = (const u16*)d_in[9];
  char* ws = (char*)d_ws;
  (void)hipFuncSetAttribute((const void*)ka_kernel, hipFuncAttributeMaxDynamicSharedMemorySize, KA_LDS);
  (void)hipFuncSetAttribute((const void*)kb_kernel, hipFuncAttributeMaxDynamicSharedMemorySize, KB_LDS);
  (void)hipFuncSetAttribute((const void*)kc_kernel, hipFuncAttributeMaxDynamicSharedMemorySize, KC_LDS);
  ka_kernel<<<248, 512, KA_LDS, stream>>>(x, wq, wk, wv, w1, b1, ws);
  kb_kernel<<<512, 256, KB_LDS, stream>>>(w2, ws);
  kc_kernel<<<288, 256, KC_LDS, stream>>>(x, wo, b2, ws, (u16*)d_out);
}
```

```cpp
#include <hip/hip_runtime.h>
#include <hip/hip_bf16.h>
#include <stdint.h>
#include <type_traits>

#if defined(__HIP_DEVICE_COMPILE__)
#pragma clang attribute push(__attribute__((target("no-packed-fp32-ops"))), apply_to = function)
#endif

typedef short bf16x8 __attribute__((ext_vector_type(8)));
typedef short bf16x4 __attribute__((ext_vector_type(4)));
typedef __bf16 bfx2 __attribute__((ext_vector_type(2)));
typedef float f32x2 __attribute__((ext_vector_type(2)));
typedef float f32x4 __attribute__((ext_vector_type(4)));
typedef float f32x16 __attribute__((ext_vector_type(16)));
typedef unsigned u32x2 __attribute__((ext_vector_type(2)));
typedef unsigned u32x4 __attribute__((ext_vector_type(4)));
typedef unsigned u32x4_a8 __attribute__((ext_vector_type(4), aligned(8)));
typedef unsigned short u16;
#define LDSP __attribute__((address_space(3)))
#define GAS __attribute__((address_space(1)))
#define DI __device__ __forceinline__
#define MFMA32(a, b, c) __builtin_amdgcn_mfma_f32_32x32x16_bf16((a), (b), (c), 0, 0, 0)
#define IC(n) std::integral_constant<int, n>{}

constexpr int SEQ = 2048, DM = 576, FF = 4608;
constexpr float LN_EPS = 1e-5f;
constexpr size_t WS_CNT = 0;
constexpr int CNT_O = 0, CNT_P = 8;
constexpr size_t WS_FLG = 8192;
constexpr size_t WS_B1F = WS_FLG + 65536;
constexpr size_t WS_HF = 131072;
constexpr size_t WS_Q = WS_HF + (size_t)64 * 288 * 1024;
constexpr size_t WS_KV = WS_Q + (size_t)SEQ * DM * 2;
constexpr size_t WS_OF = WS_KV + (size_t)64 * 2560;
constexpr size_t WS_YF = WS_OF + (size_t)64 * 36 * 1024;
constexpr size_t WS_PART = WS_YF + (size_t)64 * 36 * 1024;

DI unsigned pack2(float a, float b) {
  f32x2 p = {a, b};
  return __builtin_bit_cast(unsigned, __builtin_convertvector(p, bfx2));
}
DI float dot2(unsigned a, unsigned b, float c) {
  return __builtin_amdgcn_fdot2_f32_bf16(__builtin_bit_cast(bfx2, a), __builtin_bit_cast(bfx2, b), c, false);
}
DI float bflo(unsigned u) { return __builtin_bit_cast(float, u << 16); }
DI float bfhi(unsigned u) { return __builtin_bit_cast(float, u & 0xffff0000u); }
DI void glds16(const void* g, char* lds) { __builtin_amdgcn_global_load_lds(g, (LDSP void*)lds, 16, 0, 0); }
DI void glds16s(unsigned voff, const void* sbase, unsigned lds) {
  unsigned keep;
  asm volatile("s_mov_b32 %0, m0\n\ts_mov_b32 m0, %3\n\ts_nop 0\n\tglobal_load_lds_dwordx4 %1, %2\n\ts_mov_b32 m0, %0"
               : "=&s"(keep) : "v"(voff), "s"(sbase), "s"(lds) : "memory");
}
DI unsigned lds_addr(const char* p) { return (unsigned)(size_t)(LDSP const char*)p; }
DI bf16x8 tr_frag(const char* lds_a, const char* lds_b) {
  bf16x4 a = __builtin_amdgcn_ds_read_tr16_b64_v4i16((LDSP bf16x4*)lds_a);
  bf16x4 b = __builtin_amdgcn_ds_read_tr16_b64_v4i16((LDSP bf16x4*)lds_b);
  return __builtin_shufflevector(a, b, 0, 1, 2, 3, 4, 5, 6, 7);
}
DI bf16x8 lds_frag(const char* p) { return *(const bf16x8*)p; }
DI float xhalf(float v) {
  const int lane = __builtin_amdgcn_mbcnt_hi(~0u, __builtin_amdgcn_mbcnt_lo(~0u, 0u));
  return __builtin_bit_cast(float, __builtin_amdgcn_ds_bpermute((lane ^ 32) << 2, __builtin_bit_cast(int, v)));
}
DI float xhalf_sum(float v) { return v + xhalf(v); }
DI float xhalf_max(float v) { return __builtin_fmaxf(v, xhalf(v)); }
DI void syncthreads() {
  __builtin_amdgcn_fence(__ATOMIC_RELEASE, "workgroup");
  __builtin_amdgcn_s_barrier();
  __builtin_amdgcn_fence(__ATOMIC_ACQUIRE, "workgroup");
}
#define TID ((int)__builtin_amdgcn_workitem_id_x())
#define BID ((int)__builtin_amdgcn_workgroup_id_x())
#define WAIT_VM0() asm volatile("s_waitcnt vmcnt(0)" ::: "memory")
#define WAIT_VM(n) asm volatile("s_waitcnt vmcnt(" #n ")" ::: "memory")
#define BARRIER() __builtin_amdgcn_s_barrier()
#define SCHED_FENCE() __builtin_amdgcn_sched_barrier(0)
#define ONES2 0x3f803f80u

DI void signal(char* ws, int idx) {
  WAIT_VM0();
  syncthreads();
  if (TID == 0) __hip_atomic_fetch_add((GAS unsigned*)(ws + WS_CNT) + idx * 64, 1u, __ATOMIC_RELAXED, __HIP_MEMORY_SCOPE_AGENT);
}
DI void await(char* ws, int idx, unsigned want) {
  if (TID < 64) {
    GAS unsigned* c = (GAS unsigned*)(ws + WS_CNT) + idx * 64;
    for (unsigned spins = 0; spins < (1u << 22); ++spins) {
      if (__builtin_amdgcn_readfirstlane(__hip_atomic_load(c, __ATOMIC_RELAXED, __HIP_MEMORY_SCOPE_AGENT)) >= want) break;
      __builtin_amdgcn_s_sleep(2);
    }
  }
  syncthreads();
}
DI void frag_stats(const bf16x8& f, float& sx, float& sxx) {
  u32x4 u = __builtin_bit_cast(u32x4, f);
#pragma unroll
  for (int i = 0; i < 4; ++i) { sx = dot2(u[i], ONES2, sx); sxx = dot2(u[i], u[i], sxx); }
}
DI void frag_sum(const bf16x8& f, float& s) {
  u32x4 u = __builtin_bit_cast(u32x4, f);
#pragma unroll
  for (int i = 0; i < 4; ++i) s = dot2(u[i], ONES2, s);
}
DI void ln_affine(float sx, float sxx, float& a1, float& c1, float& a2, float& c2) {
  const float m = sx * (1.0f / DM);
  float v = sxx * (1.0f / DM) - m * m;
  v = __builtin_fmaxf(v, 0.0f);
  const float r1 = 1.0f / __builtin_sqrtf(v + LN_EPS);
  const float v2 = v * r1 * r1;
  const float r2 = 1.0f / __builtin_sqrtf(v2 + LN_EPS);
  a1 = r1; c1 = -m * r1;
  a2 = r1 * r2; c2 = -m * a2;
}
DI float vadd(float a, float b) { return a + b; }
DI float gelu_tanh(float x, float k0, float k1) {
  const float z = x * __builtin_fmaf(x * x, k1, k0);
  return x * __builtin_amdgcn_rcpf(1.0f + __builtin_amdgcn_exp2f(z));
}

constexpr int K0_W = 73728;
constexpr int K0_SCR = 159744;
constexpr int K0_LDS = 162816;

DI void k0_q_tile(char* smem, const u16* __restrict__ x, const u16* __restrict__ wq, int tb, int cg,
                  char* __restrict__ ws) {
  const int tid = TID, l = tid & 63;
  const int w = __builtin_amdgcn_readfirstlane(tid >> 6);
  const int n = w & 1, m = w >> 1;
  const int r = l & 31, h = l >> 5;
  const char* xsrc = (const char*)x + (size_t)tb * 64 * 1152;
  const char* wsrc = (const char*)wq + cg * 192;
  auto xpiece = [&](int pi) {
    const int row = (pi & 7) * 8 + (l >> 3), ch = (l & 7) ^ ((row >> 1) & 7);
    glds16(xsrc + (size_t)row * 1152 + (pi >> 3) * 128 + ch * 16, smem + pi * 1024);
  };
  unsigned woff[2];
#pragma unroll
  for (int i = 0; i < 2; ++i) {
    const int idx = (2 * w + i) * 64 + l, row = idx / 12, c = idx - row * 12;
    woff[i] = row * 1152 + c * 16;
  }
  auto wchunk = [&](int kt) {
    const int slot = kt < 7 ? kt : kt - 7;
#pragma unroll
    for (int i = 0; i < 2; ++i)
      glds16(wsrc + (size_t)kt * 64 * 1152 + woff[i], smem + K0_W + slot * 12288 + (2 * w + i) * 1024);
  };
#pragma unroll
  for (int g = 0; g < 3; ++g) {
#pragma unroll
    for (int i = 0; i < 4; ++i) xpiece(g * 24 + i * 6 + w);
    wchunk(3 * g);
    if (g < 2) { wchunk(3 * g + 1); wchunk(3 * g + 2); }
  }
  const int g4 = l >> 4, q = (l & 15) >> 2, p = l & 3;
  const int aoff = K0_W + (8 * (g4 >> 1) + q) * 192 + m * 64 + (g4 & 1) * 32 + p * 8;
  int boff[4];
#pragma unroll
  for (int ks = 0; ks < 4; ++ks) boff[ks] = n * 4096 + r * 128 + (((2 * ks + h) ^ ((r >> 1) & 7)) << 4);
  f32x16 acc;
#pragma unroll
  for (int i = 0; i < 16; ++i) acc[i] = 0.f;
  float sx = 0.f, sxx = 0.f, cs = 0.f;
  auto chunk = [&](int kt) {
    const int slot = kt < 7 ? kt : kt - 7;
    bf16x8 wf[4], xf[4];
#pragma unroll
    for (int ks = 0; ks < 4; ++ks) {
      const char* ap = smem + aoff + slot * 12288 + ks * 3072;
      wf[ks] = tr_frag(ap, ap + 768);
      xf[ks] = lds_frag(smem + kt * 8192 + boff[ks]);
    }
#pragma unroll
    for (int ks = 0; ks < 4; ++ks) acc = MFMA32(wf[ks], xf[ks], acc);
    if (kt % 3 == m) {
#pragma unroll
      for (int ks = 0; ks < 4; ++ks) frag_stats(xf[ks], sx, sxx);
    }
    frag_sum(n ? wf[2] : wf[0], cs);
    frag_sum(n ? wf[3] : wf[1], cs);
    asm volatile("" : "+v"(sx), "+v"(sxx), "+v"(cs));
  };
  WAIT_VM(16);
  BARRIER();
  chunk(0); chunk(1); chunk(2);
  __builtin_amdgcn_s_waitcnt(0xc07f);
  BARRIER();
  wchunk(7); wchunk(8);
  WAIT_VM(10);
  BARRIER();
  chunk(3); chunk(4); chunk(5);
  WAIT_VM0();
  BARRIER();
  chunk(6); chunk(7); chunk(8);
  f32x2* st = (f32x2*)(smem + K0_SCR);
  float* csp = (float*)(smem + K0_SCR + 1536);
  sx = xhalf_sum(sx); sxx = xhalf_sum(sxx); cs = xhalf_sum(cs);
  if (h == 0) { st[(m * 2 + n) * 32 + r] = (f32x2){sx, sxx}; csp[n * 96 + m * 32 + r] = cs; }
  syncthreads();
  {
    const f32x2 s0 = st[n * 32 + r], s1 = st[(2 + n) * 32 + r], s2 = st[(4 + n) * 32 + r];
    sx = s0[0] + s1[0] + s2[0]; sxx = s0[1] + s1[1] + s2[1];
  }
  float a1, c1, a2, c2;
  ln_affine(sx, sxx, a1, c1, a2, c2);
  {
    const float qs = 1.4426950408889634f * 0.23570226039551584f;
    const float a = a1 * qs, c = c1 * qs;
    char* dst = ws + WS_Q + ((size_t)(tb * 64 + n * 32 + r) * DM + cg * 96 + m * 32 + 4 * h) * 2;
#pragma unroll
    for (int g = 0; g < 4; ++g) {
      const f32x4 ca = *(const f32x4*)&csp[m * 32 + 8 * g + 4 * h], cb = *(const f32x4*)&csp[96 + m * 32 + 8 * g + 4 * h];
      float v[4];
#pragma unroll
      for (int e = 0; e < 4; ++e) v[e] = __builtin_fmaf(a, acc[4 * g + e], c * (ca[e] + cb[e]));
      u32x2 u = {pack2(v[0], v[1]), pack2(v[2], v[3])};
      *(u32x2*)(dst + g * 16) = u;
    }
  }
#pragma unroll
  for (int i = 0; i < 2; ++i) {
    const int kst = cg * 6 + m * 2 + i;
    const u32x4 u = *(const u32x4*)(smem + (kst >> 2) * 8192 + n * 4096 + r * 128 + (((2 * (kst & 3) + h) ^ ((r >> 1) & 7)) << 4));
    u32x4 o;
#pragma unroll
    for (int e = 0; e < 4; ++e) o[e] = pack2(__builtin_fmaf(a2, bflo(u[e]), c2), __builtin_fmaf(a2, bfhi(u[e]), c2));
    *(u32x4*)(ws + WS_YF + ((size_t)(tb * 2 + n) * 36 + kst) * 1024 + l * 16) = o;
  }
}

DI void k0_kv_tile(char* smem, const u16* __restrict__ x, const u16* __restrict__ wk, const u16* __restrict__ wv,
                   int tb, char* __restrict__ ws) {
  const int tid = TID, l = tid & 63;
  const int w = __builtin_amdgcn_readfirstlane(tid >> 6);
  const int r = l & 31, h = l >> 5;
  char* wimg = smem + K0_W;
  {
    const char* xsrc = (const char*)x + (size_t)tb * 64 * 1152;
#pragma unroll
    for (int i = 0; i < 12; ++i) {
      const int pi = i * 6 + w, row = (pi & 7) * 8 + (l >> 3), ch = (l & 7) ^ ((row >> 1) & 7);
      glds16(xsrc + (size_t)row * 1152 + (pi >> 3) * 128 + ch * 16, smem + pi * 1024);
    }
    unsigned v[27];
#pragma unroll
    for (int i = 0; i < 27; ++i) {
      const int idx = tid + i * 384;
      const int mat = idx >= 5184, rem = idx - mat * 5184;
      v[i] = ((const unsigned*)(mat ? wv : wk))[rem];
    }
#pragma unroll
    for (int i = 0; i < 27; ++i) {
      const int idx = tid + i * 384;
      const int mat = idx >= 5184, rem = idx - mat * 5184, row = rem / 9, dw = rem - row * 9;
      *(unsigned*)(wimg + row * 128 + ((mat ^ ((row >> 1) & 1)) * 64) + dw * 4) = v[i];
    }
  }
  WAIT_VM0();
  syncthreads();
  if (w >= 4) return;
  const int mat = w >> 1, t = w & 1;
  const int g = l >> 4, q = (l & 15) >> 2, p = l & 3;
  const int aoff = (8 * (g >> 1) + q) * 128 + (g & 1) * 32 + p * 8 + ((mat * 64) ^ (((q >> 1) & 1) * 64));
  int boff[4];
#pragma unroll
  for (int ks = 0; ks < 4; ++ks) boff[ks] = t * 4096 + r * 128 + (((2 * ks + h) ^ ((r >> 1) & 7)) << 4);
  f32x16 acc;
#pragma unroll
  for (int i = 0; i < 16; ++i) acc[i] = 0.f;
  float sx = 0.f, sxx = 0.f, cs = 0.f;
  bf16x8 wf[2][4], xf[2][4];
  auto rd = [&](int b, int kt) {
#pragma unroll
    for (int ks = 0; ks < 4; ++ks) {
      const char* ap = wimg + kt * 8192 + ks * 2048 + aoff;
      wf[b][ks] = tr_frag(ap, ap + 512);
      xf[b][ks] = lds_frag(smem + kt * 8192 + boff[ks]);
    }
  };
  auto kloop = [&](auto matc) {
    constexpr int MAT = decltype(matc)::value;
    rd(0, 0);
#pragma unroll
    for (int kt = 0; kt < 9; ++kt) {
      const int b = kt & 1;
      asm volatile("" : "+v"(wf[b][0]), "+v"(wf[b][1]), "+v"(wf[b][2]), "+v"(wf[b][3]), "+v"(xf[b][0]),
                   "+v"(xf[b][1]), "+v"(xf[b][2]), "+v"(xf[b][3]));
      SCHED_FENCE();
      if (kt + 1 < 9) rd(b ^ 1, kt + 1);
      SCHED_FENCE();
#pragma unroll
      for (int ks = 0; ks < 4; ++ks) {
        if (MAT == 0) acc = MFMA32(wf[b][ks], xf[b][ks], acc); else acc = MFMA32(xf[b][ks], wf[b][ks], acc);
        frag_stats(xf[b][ks], sx, sxx);
        frag_sum(wf[b][ks], cs);
      }
      asm volatile("" : "+v"(sx), "+v"(sxx), "+v"(cs));
      SCHED_FENCE();
    }
  };
  if (mat == 0) kloop(IC(0)); else kloop(IC(1));
  sx = xhalf_sum(sx); sxx = xhalf_sum(sxx); cs = xhalf_sum(cs);
  float a1, c1, a2, c2;
  ln_affine(sx, sxx, a1, c1, a2, c2);
  float* scr = (float*)(smem + K0_SCR + w * 256);
  char* img = ws + WS_KV + (size_t)(tb * 2 + t) * 2560;
  if (mat == 0) {
    if (h == 0) scr[r] = cs;
    __builtin_amdgcn_s_waitcnt(0xc07f);
    __builtin_amdgcn_wave_barrier();
    float v[12];
#pragma unroll
    for (int gg = 0; gg < 3; ++gg) {
      const f32x4 c4 = *(const f32x4*)&scr[8 * gg + 4 * h];
#pragma unroll
      for (int e = 0; e < 4; ++e) v[4 * gg + e] = __builtin_fmaf(a1, acc[4 * gg + e], c1 * c4[e]);
    }
    u32x4 u = {pack2(v[0], v[1]), pack2(v[2], v[3]), pack2(v[4], v[5]), pack2(v[6], v[7])};
    *(u32x4*)(img + l * 16) = u;
    if (h == 0) {
      u32x2 u2 = {pack2(v[8], v[9]), 0x00003f80u};
      *(u32x2*)(img + 1024 + r * 8) = u2;
    }
  } else {
    if (h == 0) { scr[r] = a1; scr[32 + r] = c1; }
    __builtin_amdgcn_s_waitcnt(0xc07f);
    __builtin_amdgcn_wave_barrier();
    float v[16];
#pragma unroll
    for (int gg = 0; gg < 4; ++gg) {
      const f32x4 a4 = *(const f32x4*)&scr[8 * gg + 4 * h];
      const f32x4 c4 = *(const f32x4*)&scr[32 + 8 * gg + 4 * h];
#pragma unroll
      for (int e = 0; e < 4; ++e) v[4 * gg + e] = __builtin_fmaf(a4[e], acc[4 * gg + e], c4[e] * cs);
    }
    if (r < 20) {
#pragma unroll
      for (int s = 0; s < 2; ++s) {
        u32x4 u = {pack2(v[8 * s], v[8 * s + 1]), pack2(v[8 * s + 2], v[8 * s + 3]),
                   pack2(v[8 * s + 4], v[8 * s + 5]), pack2(v[8 * s + 6], v[8 * s + 7])};
        if (r == 18) u = (u32x4){ONES2, ONES2, ONES2, ONES2};
        if (r == 19) u = (u32x4){0u, 0u, 0u, 0u};
        *(u32x4*)(img + 1280 + ((s * 2 + h) * 20 + r) * 16) = u;
      }
    }
  }
}

__global__ __launch_bounds__(384, 2) void k0_kernel(const u16* __restrict__ x, const u16* __restrict__ wq,
                                                    const u16* __restrict__ wk, const u16* __restrict__ wv,
                                                    const u16* __restrict__ b1, char* __restrict__ ws) {
  extern __shared__ __attribute__((aligned(16))) char smem[];
  const int b = BID, xcd = b & 7, j = b >> 3;
  const int tb = xcd * 4 + (j & 3), cg = j >> 2;
  if (b == 0 && TID < 32) *(unsigned*)(ws + WS_CNT + TID * 256) = 0u;
  if (b >= 16 && b < 20 && TID < 256) *(unsigned*)(ws + WS_FLG + ((b - 16) * 256 + TID) * 64) = 0u;
  if (b >= 1 && b <= 12) ((float*)(ws + WS_B1F))[(b - 1) * 384 + TID] = bflo(b1[(b - 1) * 384 + TID]);
  if (cg < 6) k0_q_tile(smem, x, wq, tb, cg, ws);
  else k0_kv_tile(smem, x, wk, wv, tb, ws);
}

constexpr int K1_LDS = 73728 + 2048 + 64;

struct KFrag { bf16x8 k0; u32x4 k1; };
struct VFrag { bf16x8 v0, v1; };

DI void attn_loop_safe(char* smem, const char* __restrict__ kvimg, int w, int l, const bf16x8 (&q0)[2],
                       const u32x4 (&q1)[2], f32x16 (&o)[2]) {
  const int r = l & 31, h = l >> 5;
  const int dv = (r == 22) ? 18 : (r >= 20 ? 19 : r);
  const int koff = l * 16, k1off = 1024 + r * 8, voff = 1280 + (h * 20 + dv) * 16;
  auto stage = [&](int c) {
#pragma unroll
    for (int i = 0; i < 5; ++i)
      glds16(kvimg + (size_t)c * 20480 + (5 * w + i) * 1024 + l * 16, smem + (c & 1) * 20480 + (5 * w + i) * 1024);
  };
  float mrun[2] = {-3.0e38f, -3.0e38f};
#pragma unroll
  for (int qt = 0; qt < 2; ++qt)
#pragma unroll
    for (int i = 0; i < 16; ++i) o[qt][i] = 0.f;
  const f32x16 zero = {};
  stage(0);
#pragma unroll 1
  for (int c = 0; c < 8; ++c) {
    WAIT_VM0();
    BARRIER();
    if (c + 1 < 8) stage(c + 1);
    const char* sb = smem + (c & 1) * 20480;
#pragma unroll 1
    for (int t = 0; t < 8; ++t) {
      const char* tb = sb + t * 2560;
      const bf16x8 kf0 = lds_frag(tb + koff);
      const u32x2 k1 = *(const u32x2*)(tb + k1off);
      const u32x4 k1v = {k1[0], k1[1], 0u, 0u};
      const bf16x8 vf0 = lds_frag(tb + voff), vf1 = lds_frag(tb + voff + 640);
#pragma unroll
      for (int qt = 0; qt < 2; ++qt) {
        f32x16 s = MFMA32(kf0, q0[qt], zero);
        s = MFMA32(__builtin_bit_cast(bf16x8, k1v), __builtin_bit_cast(bf16x8, q1[qt]), s);
        float m = s[0];
#pragma unroll
        for (int i = 1; i < 16; ++i) m = __builtin_fmaxf(m, s[i]);
        m = xhalf_max(m);
        const float mn = __builtin_fmaxf(mrun[qt], m);
        const float alpha = __builtin_amdgcn_exp2f(mrun[qt] - mn);
        mrun[qt] = mn;
#pragma unroll
        for (int i = 0; i < 16; ++i) { o[qt][i] *= alpha; s[i] -= mn; }
        unsigned p[8];
#pragma unroll
        for (int i = 0; i < 8; ++i)
          p[i] = pack2(__builtin_amdgcn_exp2f(s[2 * i]), __builtin_amdgcn_exp2f(s[2 * i + 1]));
        const u32x4 pa = {p[0], p[1], p[2], p[3]}, pb = {p[4], p[5], p[6], p[7]};
        o[qt] = MFMA32(vf0, __builtin_bit_cast(bf16x8, pa), o[qt]);
        o[qt] = MFMA32(vf1, __builtin_bit_cast(bf16x8, pb), o[qt]);
      }
    }
  }
}

DI void attn_loop_fast(char* smem, const char* __restrict__ kvimg, int w, int l, const bf16x8 (&q0)[2],
                       u32x4 (&q1)[2], f32x16 (&o)[2]) {
  const int r = l & 31, h = l >> 5;
  const int dv = (r == 22) ? 18 : (r >= 20 ? 19 : r);
  const int koff = l * 16, k1off = 1024 + r * 8, voff = 1280 + (h * 20 + dv) * 16;
  const char* gsrc = kvimg + 5 * w * 1024;
  const unsigned lbase = lds_addr(smem) + 5 * w * 1024, loff = l * 16;
  auto piece = [&](int c, int buf, int i) {
    glds16s(loff, gsrc + (size_t)c * 20480 + i * 1024, lbase + buf * 20480 + i * 1024);
  };
  const f32x16 zero = {};
  auto rdk = [&](KFrag& k, const char* tb) {
    k.k0 = lds_frag(tb + koff);
    k.k1 = *(const u32x4_a8*)(tb + k1off);
  };
  auto rdv = [&](VFrag& v, const char* tb) { v.v0 = lds_frag(tb + voff); v.v1 = lds_frag(tb + voff + 640); };
  auto qk = [&](const KFrag& k, int qt) {
    f32x16 s = MFMA32(k.k0, q0[qt], zero);
    return MFMA32(__builtin_bit_cast(bf16x8, k.k1), __builtin_bit_cast(bf16x8, q1[qt]), s);
  };
#pragma unroll
  for (int qt = 0; qt < 2; ++qt)
#pragma unroll
    for (int i = 0; i < 16; ++i) o[qt][i] = 0.f;
#pragma unroll
  for (int i = 0; i < 5; ++i) piece(0, 0, i);
  piece(1, 1, 0);
  WAIT_VM(1);
  BARRIER();
  KFrag kf[2];
  VFrag vf[2];
  rdk(kf[0], smem);
#pragma unroll
  for (int qt = 0; qt < 2; ++qt) {
    const f32x16 s = qk(kf[0], qt);
    float m = s[0];
#pragma unroll
    for (int i = 1; i < 16; ++i) m = __builtin_fmaxf(m, s[i]);
    m = xhalf_max(m);
    if (h == 0) q1[qt][1] = pack2(-m, 0.f);
  }
  f32x16 sA = qk(kf[0], 0), sB;
  u32x4 pA[2], pB[2];
  pB[0] = (u32x4){0u, 0u, 0u, 0u}; pB[1] = pB[0];
  vf[1].v0 = __builtin_bit_cast(bf16x8, pB[0]); vf[1].v1 = vf[1].v0;
  auto expack = [&](const f32x16& s, u32x4 (&p)[2]) {
#pragma unroll
    for (int i = 0; i < 8; ++i)
      p[i >> 2][i & 3] = pack2(__builtin_amdgcn_exp2f(s[2 * i]), __builtin_amdgcn_exp2f(s[2 * i + 1]));
  };
  auto tile = [&](KFrag& kc, KFrag& kn, VFrag& vp, VFrag& vc, const char* tcur, const char* tnext, bool dma, int pc,
                  int pbuf, int pi) {
    {
      rdk(kn, tnext);
      rdv(vc, tcur);
      f32x16 s = MFMA32(kc.k0, q0[1], zero);
      o[1] = MFMA32(vp.v0, __builtin_bit_cast(bf16x8, pB[0]), o[1]);
      sB = MFMA32(__builtin_bit_cast(bf16x8, kc.k1), __builtin_bit_cast(bf16x8, q1[1]), s);
      o[1] = MFMA32(vp.v1, __builtin_bit_cast(bf16x8, pB[1]), o[1]);
      expack(sA, pA);
#pragma unroll
      for (int i = 0; i < 4; ++i) {
        __builtin_amdgcn_sched_group_barrier(0x008, 1, 0);
        if (i < 2) __builtin_amdgcn_sched_group_barrier(0x100, 2, 0);
        __builtin_amdgcn_sched_group_barrier(0x400, 4, 0);
        __builtin_amdgcn_sched_group_barrier(0x002, 2, 0);
      }
      asm volatile("" : "+v"(pA[0]), "+v"(pA[1]));
    }
    SCHED_FENCE();
    {
      f32x16 s = MFMA32(kn.k0, q0[0], zero);
      o[0] = MFMA32(vc.v0, __builtin_bit_cast(bf16x8, pA[0]), o[0]);
      sA = MFMA32(__builtin_bit_cast(bf16x8, kn.k1), __builtin_bit_cast(bf16x8, q1[0]), s);
      o[0] = MFMA32(vc.v1, __builtin_bit_cast(bf16x8, pA[1]), o[0]);
      expack(sB, pB);
#pragma unroll
      for (int i = 0; i < 4; ++i) {
        __builtin_amdgcn_sched_group_barrier(0x008, 1, 0);
        __builtin_amdgcn_sched_group_barrier(0x400, 4, 0);
        __builtin_amdgcn_sched_group_barrier(0x002, 2, 0);
      }
      asm volatile("" : "+v"(pB[0]), "+v"(pB[1]));
    }
    SCHED_FENCE();
    if (dma) piece(pc, pbuf, pi);
    SCHED_FENCE();
  };
  int buf = 0;
#pragma unroll 1
  for (int c = 0; c < 8; ++c) {
    const int nbuf = buf == 2 ? 0 : buf + 1, nnbuf = nbuf == 2 ? 0 : nbuf + 1;
    const char* sb = smem + buf * 20480;
    const bool pre = c < 7;
    tile(kf[0], kf[1], vf[1], vf[0], sb, sb + 2560, pre, c + 1, nbuf, 1);
    tile(kf[1], kf[0], vf[0], vf[1], sb + 2560, sb + 5120, pre, c + 1, nbuf, 2);
    tile(kf[0], kf[1], vf[1], vf[0], sb + 5120, sb + 7680, pre, c + 1, nbuf, 3);
    tile(kf[1], kf[0], vf[0], vf[1], sb + 7680, sb + 10240, pre, c + 1, nbuf, 4);
    tile(kf[0], kf[1], vf[1], vf[0], sb + 10240, sb + 12800, false, 0, 0, 0);
    tile(kf[1], kf[0], vf[0], vf[1], sb + 12800, sb + 15360, false, 0, 0, 0);
    tile(kf[0], kf[1], vf[1], vf[0], sb + 15360, sb + 17920, false, 0, 0, 0);
    if (pre) { WAIT_VM0(); BARRIER(); }
    tile(kf[1], kf[0], vf[0], vf[1], sb + 17920, smem + nbuf * 20480, c < 6, c + 2, nnbuf, 0);
    buf = nbuf;
  }
  o[1] = MFMA32(vf[1].v0, __builtin_bit_cast(bf16x8, pB[0]), o[1]);
  o[1] = MFMA32(vf[1].v1, __builtin_bit_cast(bf16x8, pB[1]), o[1]);
}

DI void k1_attn(char* smem, int id, char* __restrict__ ws) {
  const int tid = TID, l = tid & 63;
  const int w = __builtin_amdgcn_readfirstlane(tid >> 6);
  const int r = l & 31, h = l >> 5;
  const int head = id >> 3, tblk = id & 7;
  const int tt0 = tblk * 8 + w * 2;
  bf16x8 q0[2];
  u32x4 q1[2];
#pragma unroll
  for (int qt = 0; qt < 2; ++qt) {
    const unsigned* qp = (const unsigned*)(ws + WS_Q + ((size_t)((tt0 + qt) * 32 + r) * DM + head * 18) * 2);
    u32x4 u = {qp[2 * h], qp[2 * h + 1], qp[4 + 2 * h], qp[5 + 2 * h]};
    q0[qt] = __builtin_bit_cast(bf16x8, u);
    const unsigned e = qp[8];
    q1[qt] = (u32x4){h == 0 ? e : 0u, 0u, 0u, 0u};
  }
  f32x16 o[2];
  const char* kvimg = ws + WS_KV;
  attn_loop_fast(smem, kvimg, w, l, q0, q1, o);
  bool bad = false;
#pragma unroll
  for (int qt = 0; qt < 2; ++qt) {
    bad |= !(o[qt][10] <= 1.8446744e19f);
#pragma unroll
    for (int i = 0; i < 10; ++i) bad |= !(__builtin_fabsf(o[qt][i]) <= 3.0e38f);
  }
  volatile LDSP unsigned* flag = (volatile LDSP unsigned*)(smem + 73728 + 2048);
  if (tid == 0) *flag = 0u;
  syncthreads();
  if (bad) *flag = 1u;
  syncthreads();
  if (__builtin_amdgcn_readfirstlane(*flag) != 0u) {
    q1[0][1] = 0u; q1[1][1] = 0u;
    attn_loop_safe(smem, kvimg, w, l, q0, q1, o);
  }
  const auto rs = __builtin_amdgcn_make_buffer_rsrc(ws + WS_OF, 0, 64 * 36 * 1024, 0x00020000);
#pragma unroll
  for (int qt = 0; qt < 2; ++qt) {
    const float inv = 1.0f / o[qt][10];
    const unsigned dst = (tt0 + qt) * 36 * 1024;
    u32x4 u = {pack2(o[qt][0] * inv, o[qt][1] * inv), pack2(o[qt][2] * inv, o[qt][3] * inv),
               pack2(o[qt][4] * inv, o[qt][5] * inv), pack2(o[qt][6] * inv, o[qt][7] * inv)};
    __builtin_amdgcn_raw_buffer_store_b128(u, rs, dst + head * 1024 + l * 16, 0, 16);
    if (h == 0)
      __builtin_amdgcn_raw_buffer_store_b32(pack2(o[qt][8] * inv, o[qt][9] * inv), rs,
          dst + (32 + (head >> 3)) * 1024 + (((head >> 2) & 1) * 32 + r) * 16 + (head & 3) * 4, 0, 16);
  }
  signal(ws, CNT_O + tblk);
}

DI void k1_mlp(char* smem, int id, const u16* __restrict__ w1, const u16* __restrict__ w2,
               const u16* __restrict__ wo, const u16* __restrict__ x, const u16* __restrict__ b2,
               char* __restrict__ ws, u16* __restrict__ out) {
  const int tid = TID, l = tid & 63;
  const int w = __builtin_amdgcn_readfirstlane(tid >> 6);
  const int xcd = id & 7, j = id >> 3, rh = xcd >> 2, sq = xcd & 3;
  const int rb = rh * 8 + (j & 7), cb = sq * 4 + (j >> 3), nh = cb & 1;
  const int ttile = rb * 4 + w;
  const int h = l >> 5;
  const bool five = w < 2;
  unsigned woff1[5], woff2[5];
#pragma unroll
  for (int jj = 0; jj < 5; ++jj) {
    const int idx = (w + 4 * jj < 17 ? w + 4 * jj : 17) * 64 + l;
    const unsigned prow = idx / 36, pcol = (idx - prow * 36) * 16;
    woff1[jj] = prow * 9216 + pcol; woff2[jj] = prow * 1152 + pcol;
  }
  const char* w1src = (const char*)w1 + cb * 576;
  const char* w2src = (const char*)w2 + nh * 576;
  const char* ysrc = ws + WS_YF + (size_t)ttile * 36 * 1024;
  const char* hsrc = ws + WS_HF + (size_t)ttile * 288 * 1024;
  const unsigned loff = l * 16;
  const unsigned lbase = lds_addr(smem) + w * 1024;
  auto fblk = [&](int st) { return st < 9 ? cb * 9 + st : (cb ^ 1) * 9 + st - 9; };
  auto wpiece = [&](int ph, int st, int slot, int jj) {
    const char* g = ph == 1 ? w1src + (size_t)st * 32 * 9216 : w2src + (size_t)fblk(st) * 32 * 1152;
    if (jj < 4 || five) glds16s(ph == 1 ? woff1[jj] : woff2[jj], g, lbase + slot * 18432 + jj * 4096);
  };
  auto bload = [&](int ph, int st, u32x4 (&d)[2]) {
    if (ph == 1) {
      const char* p = ysrc + st * 2048;
      asm volatile("global_load_dwordx4 %0, %2, %3\n\tglobal_load_dwordx4 %1, %2, %3 offset:1024"
                   : "=&v"(d[0]), "=&v"(d[1]) : "v"(loff), "s"(p) : "memory");
    } else {
      const char* p = hsrc + (size_t)fblk(st) * 2048;
      asm volatile("global_load_dwordx4 %0, %2, %3 sc1\n\tglobal_load_dwordx4 %1, %2, %3 offset:1024 sc1"
                   : "=&v"(d[0]), "=&v"(d[1]) : "v"(loff), "s"(p) : "memory");
    }
  };
  const int g = l >> 4, q = (l & 15) >> 2, p = l & 3;
  const int aoff1 = (8 * h + q) * 576 + (g & 1) * 32 + p * 8;
  const int aoff2 = (4 * h + q) * 576 + (g & 1) * 32 + p * 8;
  f32x16 acc[9];
  bf16x8 fa[5], fb[4];
  u32x4 br[4][2];
  auto rd = [&](const char* sb, int m, int d2) { return tr_frag(sb + m * 64, sb + m * 64 + d2); };
  auto read_a = [&](const char* sb, int d2) {
#pragma unroll
    for (int m = 0; m < 5; ++m) fa[m] = rd(sb, m, d2);
  };
  auto touch_a = [&]() { asm volatile("" : "+v"(fa[0]), "+v"(fa[1]), "+v"(fa[2]), "+v"(fa[3]), "+v"(fa[4])); };
  auto touch_b = [&]() { asm volatile("" : "+v"(fb[0]), "+v"(fb[1]), "+v"(fb[2]), "+v"(fb[3])); };
  auto group_a = [&](const u32x4& b, const char* sb, int d2, auto job0, auto job1) {
    const bf16x8 bb = __builtin_bit_cast(bf16x8, b);
    touch_a();
    SCHED_FENCE();
    acc[0] = MFMA32(fa[0], bb, acc[0]); SCHED_FENCE();
    fb[0] = rd(sb, 5, d2); fb[1] = rd(sb, 6, d2); SCHED_FENCE();
    acc[1] = MFMA32(fa[1], bb, acc[1]); SCHED_FENCE();
    fb[2] = rd(sb, 7, d2); fb[3] = rd(sb, 8, d2); SCHED_FENCE();
    acc[2] = MFMA32(fa[2], bb, acc[2]); SCHED_FENCE();
    job0(); SCHED_FENCE();
    acc[3] = MFMA32(fa[3], bb, acc[3]); SCHED_FENCE();
    job1(); SCHED_FENCE();
    acc[4] = MFMA32(fa[4], bb, acc[4]); SCHED_FENCE();
  };
  auto group_b = [&](const u32x4& b, const char* sb, int d2, auto rdc, auto job0, auto job1) {
    constexpr bool RD = decltype(rdc)::value;
    const bf16x8 bb = __builtin_bit_cast(bf16x8, b);
    SCHED_FENCE();
    acc[5] = MFMA32(fb[0], bb, acc[5]); SCHED_FENCE();
    if (RD) { fa[0] = rd(sb, 0, d2); fa[1] = rd(sb, 1, d2); fa[2] = rd(sb, 2, d2); } SCHED_FENCE();
    acc[6] = MFMA32(fb[1], bb, acc[6]); SCHED_FENCE();
    if (RD) { fa[3] = rd(sb, 3, d2); fa[4] = rd(sb, 4, d2); } SCHED_FENCE();
    acc[7] = MFMA32(fb[2], bb, acc[7]); SCHED_FENCE();
    job0(); SCHED_FENCE();
    acc[8] = MFMA32(fb[3], bb, acc[8]); SCHED_FENCE();
    job1(); SCHED_FENCE();
  };
  auto nojob = []() {};
  auto step = [&](int i, auto phc, auto slc, auto brc, auto wphc, int wst, auto blc, auto wtc, auto lastc) {
    constexpr int PH = decltype(phc)::value, SL = decltype(slc)::value, BR = decltype(brc)::value;
    constexpr int WPH = decltype(wphc)::value, BL = decltype(blc)::value, WT = decltype(wtc)::value;
    constexpr int LAST = decltype(lastc)::value;
    constexpr int PS = (SL + 3) & 3, D2 = PH == 1 ? 4 * 576 : 8 * 576;
    const int ao = PH == 1 ? aoff1 : aoff2;
    const char* sb = smem + SL * 18432 + ao;
    group_a(br[BR][0], sb, D2, [&]() { if (WPH) wpiece(WPH, wst, PS, 0); }, [&]() { if (WPH) wpiece(WPH, wst, PS, 1); });
    touch_b();
    group_b(br[BR][0], sb + 16 * 576, D2, std::true_type{}, [&]() { if (WPH) wpiece(WPH, wst, PS, 2); },
            [&]() { if (WPH) wpiece(WPH, wst, PS, 3); });
    group_a(br[BR][1], sb + 16 * 576, D2, [&]() { if (WPH) wpiece(WPH, wst, PS, 4); },
            [&]() { if (BL) bload(PH, i + 3, br[(BR + 3) & 3]); });
    touch_b();
    if (WT == 1) { if (five) WAIT_VM(14); else WAIT_VM(12); }
    if (WT == 2) { if (five) WAIT_VM(12); else WAIT_VM(10); }
    if (WT == 3) { if (five) WAIT_VM(10); else WAIT_VM(8); }
    if (WT == 4) { if (five) WAIT_VM(9); else WAIT_VM(8); }
    if (WT == 5) { if (five) WAIT_VM(7); else WAIT_VM(6); }
    if (WT == 6) WAIT_VM(0);
    if (!LAST) {
      asm volatile("" : "+v"(br[(BR + 1) & 3][0]), "+v"(br[(BR + 1) & 3][1]));
      BARRIER();
    }
    group_b(br[BR][1], smem + ((SL + 1) & 3) * 18432 + ao, D2, std::integral_constant<bool, !LAST>{}, nojob, nojob);
  };
#define PRIO 3
  if (PRIO & 1) __builtin_amdgcn_s_setprio(1);
  if (w == 0) glds16s(loff, ws + WS_B1F + cb * 1152, lds_addr(smem) + 73728);
  if (w == 1 && l < 8) glds16s(loff, ws + WS_B1F + cb * 1152 + 1024, lds_addr(smem) + 73728 + 1024);
#pragma unroll
  for (int st = 0; st < 3; ++st) {
#pragma unroll
    for (int jj = 0; jj < 5; ++jj) wpiece(1, st, st, jj);
    bload(1, st, br[st]);
  }
  if (five) WAIT_VM(14); else WAIT_VM(12);
  asm volatile("" : "+v"(br[0][0]), "+v"(br[0][1]));
  BARRIER();
  read_a(smem + aoff1, 4 * 576);
#pragma unroll
  for (int m = 0; m < 9; ++m)
#pragma unroll
    for (int gq = 0; gq < 4; ++gq) {
      const f32x4 bv = *(const f32x4*)(smem + 73728 + (m * 32 + 8 * gq + 4 * h) * 4);
#pragma unroll
      for (int e = 0; e < 4; ++e) acc[m][4 * gq + e] = bv[e];
    }
#define G1STEP(i, c) step(i, IC(1), IC(c), IC(c), IC(1), (i) + 3, IC(1), IC(1), IC(0))
#pragma unroll 1
  for (int i = 0; i < 12; i += 4) { G1STEP(i, 0); G1STEP(i + 1, 1); G1STEP(i + 2, 2); G1STEP(i + 3, 3); }
  G1STEP(12, 0); G1STEP(13, 1); G1STEP(14, 2);
  step(15, IC(1), IC(3), IC(3), IC(2), 0, IC(0), IC(2), IC(0));
  step(16, IC(1), IC(0), IC(0), IC(2), 1, IC(0), IC(3), IC(0));
  step(17, IC(1), IC(1), IC(1), IC(2), 2, IC(0), IC(0), IC(1));
  {
    const float k0 = -2.0f * 0.7978845608028654f * 1.4426950408889634f, k1 = k0 * 0.044715f;
    const auto rs = __builtin_amdgcn_make_buffer_rsrc(ws + WS_HF, 0, 64 * 288 * 1024, 0x00020000);
    const unsigned hbase = (ttile * 288 + cb * 18) * 1024 + l * 16;
#pragma unroll
    for (int m = 0; m < 9; ++m) {
#pragma unroll
      for (int s = 0; s < 2; ++s) {
        u32x4 u;
#pragma unroll
        for (int e = 0; e < 4; ++e)
          u[e] = pack2(gelu_tanh(acc[m][8 * s + 2 * e], k0, k1), gelu_tanh(acc[m][8 * s + 2 * e + 1], k0, k1));
        __builtin_amdgcn_raw_buffer_store_b128(u, rs, hbase + (m * 2 + s) * 1024, 0, 16);
      }
    }
  }
  WAIT_VM0();
  if (l == 0) __hip_atomic_store((GAS unsigned*)(ws + WS_FLG) + ((rb * 16 + cb) * 4 + w) * 16, 1u, __ATOMIC_RELAXED, __HIP_MEMORY_SCOPE_AGENT);
  if (PRIO & 2) __builtin_amdgcn_s_setprio(1); else __builtin_amdgcn_s_setprio(0);
#pragma unroll
  for (int st = 0; st < 3; ++st) bload(2, st, br[st]);
  BARRIER();
  read_a(smem + 2 * 18432 + aoff2, 8 * 576);
#pragma unroll
  for (int m = 0; m < 9; ++m)
#pragma unroll
    for (int i = 0; i < 16; ++i) acc[m][i] = 0.f;
  WAIT_VM(4);
  asm volatile("" : "+v"(br[0][0]), "+v"(br[0][1]));
#define G2STEP(i, c, wt) step(i, IC(2), IC(((c) + 2) & 3), IC(c), IC(2), (i) + 3, IC(1), IC(wt), IC(0))
  G2STEP(0, 0, 4); G2STEP(1, 1, 1); G2STEP(2, 2, 1); G2STEP(3, 3, 1);
  G2STEP(4, 0, 1); G2STEP(5, 1, 1);
  {
    GAS unsigned* f = (GAS unsigned*)(ws + WS_FLG) + ((rb * 16 + (cb ^ 1)) * 4 + w) * 16;
    for (unsigned spins = 0; spins < (1u << 22); ++spins) {
      if (__builtin_amdgcn_readfirstlane(__hip_atomic_load(f, __ATOMIC_RELAXED, __HIP_MEMORY_SCOPE_AGENT)) != 0u) break;
      __builtin_amdgcn_s_sleep(2);
    }
  }
  G2STEP(6, 2, 1); G2STEP(7, 3, 1);
#pragma unroll 1
  for (int i = 8; i < 12; i += 4) { G2STEP(i, 0, 1); G2STEP(i + 1, 1, 1); G2STEP(i + 2, 2, 1); G2STEP(i + 3, 3, 1); }
  G2STEP(12, 0, 1); G2STEP(13, 1, 1); G2STEP(14, 2, 1);
  step(15, IC(2), IC(1), IC(3), IC(0), 0, IC(0), IC(5), IC(0));
  step(16, IC(2), IC(2), IC(0), IC(0), 0, IC(0), IC(6), IC(0));
  step(17, IC(2), IC(3), IC(1), IC(0), 0, IC(0), IC(0), IC(1));
  const auto rp = __builtin_amdgcn_make_buffer_rsrc(ws + WS_PART, 0, 64 * 18 * 16 * 1024, 0x00020000);
#pragma unroll
  for (int m = 0; m < 9; ++m)
#pragma unroll
    for (int s = 0; s < 2; ++s) {
      const u32x4 u = {pack2(acc[m][8 * s], acc[m][8 * s + 1]), pack2(acc[m][8 * s + 2], acc[m][8 * s + 3]),
                       pack2(acc[m][8 * s + 4], acc[m][8 * s + 5]), pack2(acc[m][8 * s + 6], acc[m][8 * s + 7])};
      __builtin_amdgcn_raw_buffer_store_b128(u, rp, (((ttile * 18 + nh * 9 + m) * 2 + s) * 8 + (cb >> 1)) * 1024 + l * 16, 0, 16);
    }
  signal(ws, CNT_P + rb);
  if (j >= 18) return;
  const int rbf = rh * 8 + (sq * 18 + j) / 9, nb = (sq * 18 + j) % 9, ft = rbf * 4 + w;
#pragma unroll
  for (int i = 0; i < 18; ++i) {
    const int pc = w + 4 * i, kp = pc * 8 + (l >> 3);
    int row;
    if (i < 16) row = (kp >> 4) * 18 + (kp & 15);
    else {
      const int rr = (kp - 512) >> 4, ii = kp & 15, hh = (ii >> 2) & 1, jj = 4 * (ii >> 3) + (ii & 3);
      row = 18 * (8 * rr + 4 * hh + (jj >> 1)) + 16 + (jj & 1);
    }
    const int ch = (l & 7) ^ (((kp >> 1) & 1) << 2);
    glds16s(row * 1152 + ch * 16, (const char*)wo + nb * 128, lds_addr(smem) + pc * 1024);
  }
  const size_t obase = (size_t)(ft * 32 + (l & 31)) * DM + nb * 64 + 4 * h;
  f32x16 oa[2];
#pragma unroll
  for (int t = 0; t < 2; ++t)
#pragma unroll
    for (int gq = 0; gq < 4; ++gq) {
      const u32x2 xv = *(const u32x2*)(x + obase + t * 32 + 8 * gq);
      const u32x2 bv = *(const u32x2*)(b2 + nb * 64 + t * 32 + 4 * h + 8 * gq);
      oa[t][4 * gq + 0] = bflo(xv[0]) + bflo(bv[0]);
      oa[t][4 * gq + 1] = bfhi(xv[0]) + bfhi(bv[0]);
      oa[t][4 * gq + 2] = bflo(xv[1]) + bflo(bv[1]);
      oa[t][4 * gq + 3] = bfhi(xv[1]) + bfhi(bv[1]);
    }
  await(ws, CNT_P + rbf, 16u);
  {
    u32x4 pv[2][2][8];
#pragma unroll
    for (int t = 0; t < 2; ++t)
#pragma unroll
      for (int s = 0; s < 2; ++s)
#pragma unroll
        for (int src = 0; src < 8; ++src)
          pv[t][s][src] = __builtin_amdgcn_raw_buffer_load_b128(rp, (((ft * 18 + nb * 2 + t) * 2 + s) * 8 + src) * 1024 + l * 16, 0, 16);
#pragma unroll
    for (int t = 0; t < 2; ++t)
#pragma unroll
      for (int s = 0; s < 2; ++s)
#pragma unroll
        for (int src = 0; src < 8; ++src)
#pragma unroll
          for (int e = 0; e < 4; ++e) {
            const unsigned u = pv[t][s][src][e];
            oa[t][8 * s + 2 * e] += bflo(u);
            oa[t][8 * s + 2 * e + 1] += bfhi(u);
          }
  }
  asm volatile("" : "+v"(oa[0]), "+v"(oa[1]));
  WAIT_VM0();
  await(ws, CNT_O + (rbf >> 1), 32u);
  {
    const auto ro = __builtin_amdgcn_make_buffer_rsrc(ws + WS_OF, 0, 64 * 36 * 1024, 0x00020000);
    u32x4 of[36];
#pragma unroll
    for (int ks = 0; ks < 36; ++ks) of[ks] = __builtin_amdgcn_raw_buffer_load_b128(ro, (ft * 36 + ks) * 1024 + l * 16, 0, 16);
    const int ao = (4 * h + q) * 128 + (g & 1) * 32 + p * 8, sw = ((q >> 1) & 1) * 64;
    const char* a0 = smem + ao + sw;
    const char* a1 = smem + ao + (64 ^ sw);
    bf16x8 af[2][3][2];
    auto rdw = [&](int b, int kb) {
#pragma unroll
      for (int i = 0; i < 3; ++i) {
        const int ks = kb * 3 + i;
        af[b][i][0] = tr_frag(a0 + ks * 2048, a0 + ks * 2048 + 1024);
        af[b][i][1] = tr_frag(a1 + ks * 2048, a1 + ks * 2048 + 1024);
      }
    };
    rdw(0, 0);
#pragma unroll
    for (int kb = 0; kb < 12; ++kb) {
      const int b = kb & 1;
      asm volatile("" : "+v"(af[b][0][0]), "+v"(af[b][0][1]), "+v"(af[b][1][0]), "+v"(af[b][1][1]), "+v"(af[b][2][0]), "+v"(af[b][2][1]));
      SCHED_FENCE();
      if (kb + 1 < 12) rdw(b ^ 1, kb + 1);
      SCHED_FENCE();
#pragma unroll
      for (int i = 0; i < 3; ++i) {
        oa[0] = MFMA32(af[b][i][0], __builtin_bit_cast(bf16x8, of[kb * 3 + i]), oa[0]);
        oa[1] = MFMA32(af[b][i][1], __builtin_bit_cast(bf16x8, of[kb * 3 + i]), oa[1]);
      }
      SCHED_FENCE();
    }
  }
#pragma unroll
  for (int t = 0; t < 2; ++t)
#pragma unroll
    for (int gq = 0; gq < 4; ++gq) {
      const u32x2 u = {pack2(oa[t][4 * gq], oa[t][4 * gq + 1]), pack2(oa[t][4 * gq + 2], oa[t][4 * gq + 3])};
      *(u32x2*)(out + obase + t * 32 + 8 * gq) = u;
    }
}

__global__ __launch_bounds__(256, 2) void k1_kernel(const u16* __restrict__ w1, const u16* __restrict__ w2,
                                                    const u16* __restrict__ wo, const u16* __restrict__ x,
                                                    const u16* __restrict__ b2, char* __restrict__ ws,
                                                    u16* __restrict__ out) {
  extern __shared__ __attribute__((aligned(16))) char smem[];
  const int b = BID;
  if (b < 256) k1_attn(smem, b, ws);
  else k1_mlp(smem, b - 256, w1, w2, wo, x, b2, ws, out);
}

#if defined(__HIP_DEVICE_COMPILE__)
#pragma clang attribute pop
#endif

extern "C" void kernel_launch(void* const* d_in, const int* in_sizes, int n_in, void* d_out, int out_size,
                              void* d_ws, size_t ws_size, hipStream_t stream) {
  const u16* x = (const u16*)d_in[0];
  const u16* wq = (const u16*)d_in[1];
  const u16* wk = (const u16*)d_in[2];
  const u16* wv = (const u16*)d_in[3];
  const u16* wo = (const u16*)d_in[4];
  const u16* w1 = (const u16*)d_in[6];
  const u16* b1 = (const u16*)d_in[7];
  const u16* w2 = (const u16*)d_in[8];
  const u16* b2 = (const u16*)d_in[9];
  char* ws = (char*)d_ws;
  (void)hipFuncSetAttribute((const void*)k0_kernel, hipFuncAttributeMaxDynamicSharedMemorySize, K0_LDS);
  (void)hipFuncSetAttribute((const void*)k1_kernel, hipFuncAttributeMaxDynamicSharedMemorySize, K1_LDS);
  k0_kernel<<<224, 384, K0_LDS, stream>>>(x, wq, wk, wv, b1, ws);
  k1_kernel<<<512, 256, K1_LDS, stream>>>(w1, w2, wo, x, b2, ws, (u16*)d_out);
}
```

```cpp
#include <hip/hip_runtime.h>
#include <hip/hip_bf16.h>
#include <stdint.h>
#include <type_traits>

#if defined(__HIP_DEVICE_COMPILE__)
#pragma clang attribute push(__attribute__((target("no-packed-fp32-ops"))), apply_to = function)
#endif

typedef short bf16x8 __attribute__((ext_vector_type(8)));
typedef short bf16x4 __attribute__((ext_vector_type(4)));
typedef __bf16 bfx2 __attribute__((ext_vector_type(2)));
typedef float f32x2 __attribute__((ext_vector_type(2)));
typedef float f32x4 __attribute__((ext_vector_type(4)));
typedef float f32x16 __attribute__((ext_vector_type(16)));
typedef unsigned u32x2 __attribute__((ext_vector_type(2)));
typedef unsigned u32x4 __attribute__((ext_vector_type(4)));
typedef unsigned u32x4_a8 __attribute__((ext_vector_type(4), aligned(8)));
typedef unsigned short u16;
#define LDSP __attribute__((address_space(3)))
#define GAS __attribute__((address_space(1)))
#define DI __device__ __forceinline__
#define MFMA32(a, b, c) __builtin_amdgcn_mfma_f32_32x32x16_bf16((a), (b), (c), 0, 0, 0)
#define IC(n) std::integral_constant<int, n>{}

constexpr int SEQ = 2048, DM = 576, FF = 4608;
constexpr float LN_EPS = 1e-5f;
constexpr size_t WS_CNT = 0;
constexpr int CNT_O = 0, CNT_P = 16, CNT_KV = 32;
constexpr size_t WS_FLG = 16384;
constexpr size_t WS_B1F = WS_FLG + 65536;
constexpr size_t WS_RINV = WS_B1F + 18432;
constexpr size_t WS_HF = 131072;
constexpr size_t WS_KV = WS_HF + (size_t)64 * 288 * 1024;
constexpr size_t WS_OF = WS_KV + (size_t)64 * 2560;
constexpr size_t WS_YF = WS_OF + (size_t)64 * 36 * 1024;
constexpr size_t WS_PART = WS_YF + (size_t)64 * 36 * 1024;

DI unsigned pack2(float a, float b) {
  f32x2 p = {a, b};
  return __builtin_bit_cast(unsigned, __builtin_convertvector(p, bfx2));
}
DI float bflo(unsigned u) { return __builtin_bit_cast(float, u << 16); }
DI float bfhi(unsigned u) { return __builtin_bit_cast(float, u & 0xffff0000u); }
DI void glds16(const void* g, char* lds) { __builtin_amdgcn_global_load_lds(g, (LDSP void*)lds, 16, 0, 0); }
DI void glds16s(unsigned voff, const void* sbase, unsigned lds) {
  unsigned keep;
  asm volatile("s_mov_b32 %0, m0\n\ts_mov_b32 m0, %3\n\ts_nop 0\n\tglobal_load_lds_dwordx4 %1, %2\n\ts_mov_b32 m0, %0"
               : "=&s"(keep) : "v"(voff), "s"(sbase), "s"(lds) : "memory");
}
DI unsigned lds_addr(const char* p) { return (unsigned)(size_t)(LDSP const char*)p; }
DI bf16x8 tr_frag(const char* lds_a, const char* lds_b) {
  bf16x4 a = __builtin_amdgcn_ds_read_tr16_b64_v4i16((LDSP bf16x4*)lds_a);
  bf16x4 b = __builtin_amdgcn_ds_read_tr16_b64_v4i16((LDSP bf16x4*)lds_b);
  return __builtin_shufflevector(a, b, 0, 1, 2, 3, 4, 5, 6, 7);
}
DI bf16x8 lds_frag(const char* p) { return *(const bf16x8*)p; }
DI float xhalf(float v) {
  const int lane = __builtin_amdgcn_mbcnt_hi(~0u, __builtin_amdgcn_mbcnt_lo(~0u, 0u));
  return __builtin_bit_cast(float, __builtin_amdgcn_ds_bpermute((lane ^ 32) << 2, __builtin_bit_cast(int, v)));
}
DI float xhalf_sum(float v) { return v + xhalf(v); }
DI float xhalf_max(float v) { return __builtin_fmaxf(v, xhalf(v)); }
DI void syncthreads() {
  __builtin_amdgcn_fence(__ATOMIC_RELEASE, "workgroup");
  __builtin_amdgcn_s_barrier();
  __builtin_amdgcn_fence(__ATOMIC_ACQUIRE, "workgroup");
}
#define TID ((int)__builtin_amdgcn_workitem_id_x())
#define BID ((int)__builtin_amdgcn_workgroup_id_x())
#define WAIT_VM0() asm volatile("s_waitcnt vmcnt(0)" ::: "memory")
#define WAIT_VM(n) asm volatile("s_waitcnt vmcnt(" #n ")" ::: "memory")
#define BARRIER() __builtin_amdgcn_s_barrier()
#define SCHED_FENCE() __builtin_amdgcn_sched_barrier(0)
#define ONES2 0x3f803f80u

DI void signal(char* ws, int idx) {
  WAIT_VM0();
  syncthreads();
  if (TID == 0) __hip_atomic_fetch_add((GAS unsigned*)(ws + WS_CNT) + idx * 64, 1u, __ATOMIC_RELAXED, __HIP_MEMORY_SCOPE_AGENT);
}
DI void await(char* ws, int idx, unsigned want) {
  if (TID < 64) {
    GAS unsigned* c = (GAS unsigned*)(ws + WS_CNT) + idx * 64;
    for (unsigned spins = 0; spins < (1u << 22); ++spins) {
      if (__builtin_amdgcn_readfirstlane(__hip_atomic_load(c, __ATOMIC_RELAXED, __HIP_MEMORY_SCOPE_AGENT)) >= want) break;
      __builtin_amdgcn_s_sleep(8);
    }
  }
  syncthreads();
}
DI float vadd(float a, float b) { return a + b; }
DI float gelu_tanh(float x, float k0, float k1) {
  const float z = x * __builtin_fmaf(x * x, k1, k0);
  return x * __builtin_amdgcn_rcpf(1.0f + __builtin_amdgcn_exp2f(z));
}

template <int CTRL> DI float dpp_f(float v) {
  return __builtin_bit_cast(float, __builtin_amdgcn_update_dpp(0, __builtin_bit_cast(int, v), CTRL, 0xf, 0xf, true));
}
DI float sum8(float v) {
  v += dpp_f<0xB1>(v);
  v += dpp_f<0x4E>(v);
  v += dpp_f<0x141>(v);
  return v;
}
DI float dot2(unsigned a, unsigned b, float c) {
  return __builtin_amdgcn_fdot2_f32_bf16(__builtin_bit_cast(bfx2, a), __builtin_bit_cast(bfx2, b), c, false);
}
DI void k0_ln(const u16* __restrict__ x, int tok0, char* __restrict__ ws) {
  const int l = TID & 63, c8 = l & 7, tok = tok0 + (l >> 3);
  u32x4 v[9];
#pragma unroll
  for (int i = 0; i < 9; ++i) v[i] = *(const u32x4*)((const char*)x + (size_t)tok * 1152 + (c8 + 8 * i) * 16);
  float f[72];
  float sx = 0.f;
#pragma unroll
  for (int i = 0; i < 9; ++i)
#pragma unroll
    for (int e = 0; e < 4; ++e) {
      f[8 * i + 2 * e] = bflo(v[i][e]); f[8 * i + 2 * e + 1] = bfhi(v[i][e]);
      sx += f[8 * i + 2 * e] + f[8 * i + 2 * e + 1];
    }
  const float m = sum8(sx) * (1.0f / DM);
  float sq = 0.f;
#pragma unroll
  for (int i = 0; i < 72; ++i) { f[i] -= m; sq = __builtin_fmaf(f[i], f[i], sq); }
  const float var = sum8(sq) * (1.0f / DM);
  const float r1 = 1.0f / __builtin_sqrtf(var + LN_EPS);
  const float s2 = __builtin_sqrtf(var * r1 * r1 + LN_EPS);
  const float a2 = r1 / s2;
  if (c8 == 0) ((float*)(ws + WS_RINV))[tok] = s2;
  const bool odd = c8 & 1;
  char* dst = ws + WS_YF + ((size_t)(tok >> 5) * 36 + (c8 >> 1)) * 1024 + ((c8 & 1) * 32 + (tok & 31)) * 16;
#pragma unroll
  for (int i = 0; i < 9; ++i) {
    unsigned u[4];
#pragma unroll
    for (int e = 0; e < 4; ++e) u[e] = pack2(f[8 * i + 2 * e] * a2, f[8 * i + 2 * e + 1] * a2);
    const unsigned s0 = odd ? u[0] : u[2], s1 = odd ? u[1] : u[3];
    const unsigned r0 = __builtin_amdgcn_update_dpp(0, (int)s0, 0xB1, 0xf, 0xf, true);
    const unsigned r1u = __builtin_amdgcn_update_dpp(0, (int)s1, 0xB1, 0xf, 0xf, true);
    const u32x4 o = odd ? (u32x4){r0, r1u, u[2], u[3]} : (u32x4){u[0], u[1], r0, r1u};
    *(u32x4*)(dst + i * 4096) = o;
  }
}

constexpr int K0_LDS = 36864 + 73728 + 1024;
DI void k0_kv(char* smem, const u16* __restrict__ x, const u16* __restrict__ wk, const u16* __restrict__ wv, int tt,
              char* __restrict__ ws) {
  const int tid = TID, l = tid & 63;
  const int w = __builtin_amdgcn_readfirstlane(tid >> 6);
  const int r = l & 31, h = l >> 5;
  const int kq = w >> 1, mat = w & 1;
  char* wimg = smem + 36864 + w * 9216;
  {
    const int sub = (l & 3) == 2 ? 20 : (l & 1) * 16;
    const char* wsrc = (const char*)(mat ? wv : wk) + kq * 144 * 36;
#pragma unroll
    for (int i = 0; i < 9; ++i) glds16s((l >> 2) * 36 + sub, wsrc + i * 16 * 36, lds_addr(wimg) + i * 1024);
    const int row = (w & 3) * 8 + (l >> 3), ch = (l & 7) ^ ((row >> 1) & 7);
    const char* xsrc = (const char*)x + (size_t)tt * 32 * 1152 + (w >> 2) * 128;
#pragma unroll
    for (int i = 0; i < 5; ++i)
      if (i < 4 || w < 4) glds16s(row * 1152 + ch * 16, xsrc + i * 256, lds_addr(smem) + (i * 8 + w) * 1024);
  }
  WAIT_VM0();
  BARRIER();
  const int g = l >> 4, q = (l & 15) >> 2, p = l & 3;
  const int aoff = (8 * (g >> 1) + q) * 64 + (g & 1) * 32 + p * 8;
  f32x16 acc;
#pragma unroll
  for (int i = 0; i < 16; ++i) acc[i] = 0.f;
  float sx = 0.f, sxx = 0.f, cs = 0.f;
  auto kloop = [&](auto matc) {
    constexpr int MAT = decltype(matc)::value;
#pragma unroll 1
    for (int k3 = 0; k3 < 9; k3 += 3) {
      bf16x8 wf[3], xf[3];
#pragma unroll
      for (int i = 0; i < 3; ++i) {
        const int kst = kq * 9 + k3 + i;
        const char* ap = wimg + aoff + (k3 + i) * 1024;
        wf[i] = tr_frag(ap, ap + 256);
        xf[i] = lds_frag(smem + (kst >> 2) * 4096 + r * 128 + (((2 * (kst & 3) + h) ^ ((r >> 1) & 7)) << 4));
      }
#pragma unroll
      for (int i = 0; i < 3; ++i) {
        if (MAT == 0) acc = MFMA32(wf[i], xf[i], acc); else acc = MFMA32(xf[i], wf[i], acc);
      }
#pragma unroll
      for (int i = 0; i < 3; ++i) {
        const u32x4 ux = __builtin_bit_cast(u32x4, xf[i]), uw = __builtin_bit_cast(u32x4, wf[i]);
#pragma unroll
        for (int e = 0; e < 4; ++e) { sx = dot2(ux[e], ONES2, sx); sxx = dot2(ux[e], ux[e], sxx); cs = dot2(uw[e], ONES2, cs); }
      }
      asm volatile("" : "+v"(sx), "+v"(sxx), "+v"(cs));
    }
  };
  if (mat == 0) kloop(IC(0)); else kloop(IC(1));
  {
    char* red = wimg + l * 16;
    if (kq != 0) {
#pragma unroll
      for (int gq = 0; gq < 4; ++gq) *(f32x4*)(red + gq * 1024) = (f32x4){acc[4 * gq], acc[4 * gq + 1], acc[4 * gq + 2], acc[4 * gq + 3]};
      *(f32x4*)(red + 4096) = (f32x4){sx, sxx, cs, 0.f};
    }
    syncthreads();
    if (kq != 0) return;
#pragma unroll
    for (int s = 1; s < 4; ++s) {
      const char* d = red + 2 * s * 9216;
#pragma unroll
      for (int gq = 0; gq < 4; ++gq) {
        const f32x4 v = *(const f32x4*)(d + gq * 1024);
#pragma unroll
        for (int e = 0; e < 4; ++e) acc[4 * gq + e] += v[e];
      }
      const f32x4 v = *(const f32x4*)(d + 4096);
      sx += v[0]; sxx += v[1]; cs += v[2];
    }
  }
  sx = xhalf_sum(sx); sxx = xhalf_sum(sxx); cs = xhalf_sum(cs);
  const float mean = sx * (1.0f / DM);
  const float var = __builtin_fmaxf(sxx * (1.0f / DM) - mean * mean, 0.0f);
  const float a1 = 1.0f / __builtin_sqrtf(var + LN_EPS), c1 = -mean * a1;
  float* scr = (float*)(smem + 36864 + 73728 + w * 256);
  char* img = ws + WS_KV + (size_t)tt * 2560;
  if (mat == 0) {
    if (h == 0) scr[r] = cs;
    __builtin_amdgcn_s_waitcnt(0xc07f);
    __builtin_amdgcn_wave_barrier();
    float v[12];
#pragma unroll
    for (int gg = 0; gg < 3; ++gg) {
      const f32x4 c4 = *(const f32x4*)&scr[8 * gg + 4 * h];
#pragma unroll
      for (int e = 0; e < 4; ++e) v[4 * gg + e] = __builtin_fmaf(a1, acc[4 * gg + e], c1 * c4[e]);
    }
    const u32x4 u = {pack2(v[0], v[1]), pack2(v[2], v[3]), pack2(v[4], v[5]), pack2(v[6], v[7])};
    *(u32x4*)(img + l * 16) = u;
    if (h == 1) *(u32x2*)(img + 1024 + r * 8) = (u32x2){pack2(v[10], v[11]), 0x00003f80u};
  } else {
    if (h == 0) { scr[r] = a1; scr[32 + r] = c1; }
    __builtin_amdgcn_s_waitcnt(0xc07f);
    __builtin_amdgcn_wave_barrier();
    float v[16];
#pragma unroll
    for (int gg = 0; gg < 4; ++gg) {
      const f32x4 a4 = *(const f32x4*)&scr[8 * gg + 4 * h];
      const f32x4 c4 = *(const f32x4*)&scr[32 + 8 * gg + 4 * h];
#pragma unroll
      for (int e = 0; e < 4; ++e) v[4 * gg + e] = __builtin_fmaf(a4[e], acc[4 * gg + e], c4[e] * cs);
    }
    const int vr = r >= 22 ? r - 6 : r;
    if (r < 16 || r == 18 || r == 19 || r == 22 || r == 23) {
#pragma unroll
      for (int s = 0; s < 2; ++s) {
        u32x4 u = {pack2(v[8 * s], v[8 * s + 1]), pack2(v[8 * s + 2], v[8 * s + 3]),
                   pack2(v[8 * s + 4], v[8 * s + 5]), pack2(v[8 * s + 6], v[8 * s + 7])};
        if (r == 18) u = (u32x4){ONES2, ONES2, ONES2, ONES2};
        if (r == 19) u = (u32x4){0u, 0u, 0u, 0u};
        *(u32x4*)(img + 1280 + ((s * 2 + h) * 20 + vr) * 16) = u;
      }
    }
  }
}

__global__ __launch_bounds__(512, 2) void k0_kernel(const u16* __restrict__ x, const u16* __restrict__ wk,
                                                    const u16* __restrict__ wv, const u16* __restrict__ b1,
                                                    const u16* __restrict__ w1, const u16* __restrict__ wq,
                                                    char* __restrict__ ws) {
  extern __shared__ __attribute__((aligned(16))) char smem[];
  const int b = BID, tid = TID;
  if (b >= 192) {
    const int sq = b & 3, t = ((b - 192) >> 3) * 512 + tid;
    unsigned acc = 0;
#pragma unroll
    for (int i = 0; i < 3; ++i) {
      int li = t + i * 4096; li = li < 10368 ? li : 10367;
      acc ^= *(const unsigned*)((const char*)w1 + (size_t)(li / 18) * 9216 + sq * 2304 + (li % 18) * 128);
    }
#pragma unroll
    for (int i = 0; i < 2; ++i) {
      int li = t + i * 4096; li = li < 5184 ? li : 5183;
      acc ^= *(const unsigned*)((const char*)wq + (size_t)li * 128);
    }
    asm volatile("" :: "v"(acc));
  } else if (b < 64) {
    k0_kv(smem, x, wk, wv, b, ws);
  } else {
    const int i = b - 64;
    if (tid < 128) k0_ln(x, i * 16 + (tid >> 6) * 8, ws);
    else if (tid < 164) ((float*)(ws + WS_B1F))[i * 36 + tid - 128] = bflo(b1[i * 36 + tid - 128]);
    else if (tid >= 192 && tid < 200) *(unsigned*)(ws + WS_FLG + (i * 8 + tid - 192) * 64) = 0u;
    else if (i == 0 && tid >= 256 && tid < 320) *(unsigned*)(ws + WS_CNT + (tid - 256) * 256) = 0u;
  }
}

constexpr int K1_LDS = 73728 + 2048 + 64;
constexpr unsigned K1_CODE = 24576;

struct KFrag { bf16x8 k0; u32x4 k1; };
struct VFrag { bf16x8 v0, v1; };

DI void attn_loop_safe(char* smem, const char* __restrict__ kvimg, int w, int l, const bf16x8 (&q0)[2],
                       const u32x4 (&q1)[2], f32x16 (&o)[2]) {
  const int r = l & 31, h = l >> 5;
  const int dv = (r == 22) ? 18 : (r >= 20 ? 19 : r);
  const int koff = l * 16, k1off = 1024 + r * 8, voff = 1280 + (h * 20 + dv) * 16;
  auto stage = [&](int c) {
#pragma unroll
    for (int i = 0; i < 5; ++i)
      glds16s(l * 16, kvimg + (size_t)c * 20480 + (5 * w + i) * 1024, lds_addr(smem) + (c & 1) * 20480 + (5 * w + i) * 1024);
  };
  float mrun[2] = {-3.0e38f, -3.0e38f};
#pragma unroll
  for (int qt = 0; qt < 2; ++qt)
#pragma unroll
    for (int i = 0; i < 16; ++i) o[qt][i] = 0.f;
  const f32x16 zero = {};
  stage(0);
#pragma unroll 1
  for (int c = 0; c < 8; ++c) {
    WAIT_VM0();
    BARRIER();
    if (c + 1 < 8) stage(c + 1);
    const char* sb = smem + (c & 1) * 20480;
#pragma unroll 1
    for (int t = 0; t < 8; ++t) {
      const char* tb = sb + t * 2560;
      const bf16x8 kf0 = lds_frag(tb + koff);
      const u32x2 k1 = *(const u32x2*)(tb + k1off);
      const u32x4 k1v = {k1[0], k1[1], 0u, 0u};
      const bf16x8 vf0 = lds_frag(tb + voff), vf1 = lds_frag(tb + voff + 640);
#pragma unroll
      for (int qt = 0; qt < 2; ++qt) {
        f32x16 s = MFMA32(kf0, q0[qt], zero);
        s = MFMA32(__builtin_bit_cast(bf16x8, k1v), __builtin_bit_cast(bf16x8, q1[qt]), s);
        float m = s[0];
#pragma unroll
        for (int i = 1; i < 16; ++i) m = __builtin_fmaxf(m, s[i]);
        m = xhalf_max(m);
        const float mn = __builtin_fmaxf(mrun[qt], m);
        const float alpha = __builtin_amdgcn_exp2f(mrun[qt] - mn);
        mrun[qt] = mn;
#pragma unroll
        for (int i = 0; i < 16; ++i) { o[qt][i] *= alpha; s[i] -= mn; }
        unsigned p[8];
#pragma unroll
        for (int i = 0; i < 8; ++i)
          p[i] = pack2(__builtin_amdgcn_exp2f(s[2 * i]), __builtin_amdgcn_exp2f(s[2 * i + 1]));
        const u32x4 pa = {p[0], p[1], p[2], p[3]}, pb = {p[4], p[5], p[6], p[7]};
        o[qt] = MFMA32(vf0, __builtin_bit_cast(bf16x8, pa), o[qt]);
        o[qt] = MFMA32(vf1, __builtin_bit_cast(bf16x8, pb), o[qt]);
      }
    }
  }
}

DI void attn_loop_fast(char* smem, const char* __restrict__ kvimg, int w, int l, const bf16x8 (&q0)[2],
                       u32x4 (&q1)[2], f32x16 (&o)[2]) {
  const int r = l & 31, h = l >> 5;
  const int dv = (r == 22) ? 18 : (r >= 20 ? 19 : r);
  const int koff = l * 16, k1off = 1024 + r * 8, voff = 1280 + (h * 20 + dv) * 16;
  const char* gsrc = kvimg + 5 * w * 1024;
  const unsigned lbase = lds_addr(smem) + 5 * w * 1024, loff = l * 16;
  auto piece = [&](int c, int buf, int i) {
    glds16s(loff, gsrc + (size_t)c * 20480 + i * 1024, lbase + buf * 20480 + i * 1024);
  };
  const f32x16 zero = {};
  auto rdk = [&](KFrag& k, const char* tb) {
    k.k0 = lds_frag(tb + koff);
    k.k1 = *(const u32x4_a8*)(tb + k1off);
  };
  auto rdv = [&](VFrag& v, const char* tb) { v.v0 = lds_frag(tb + voff); v.v1 = lds_frag(tb + voff + 640); };
  auto qk = [&](const KFrag& k, int qt) {
    f32x16 s = MFMA32(k.k0, q0[qt], zero);
    return MFMA32(__builtin_bit_cast(bf16x8, k.k1), __builtin_bit_cast(bf16x8, q1[qt]), s);
  };
#pragma unroll
  for (int qt = 0; qt < 2; ++qt)
#pragma unroll
    for (int i = 0; i < 16; ++i) o[qt][i] = 0.f;
#pragma unroll
  for (int i = 0; i < 5; ++i) piece(0, 0, i);
  piece(1, 1, 0);
  WAIT_VM(1);
  BARRIER();
  KFrag kf[2];
  VFrag vf[2];
  rdk(kf[0], smem);
#pragma unroll
  for (int qt = 0; qt < 2; ++qt) {
    const f32x16 s = qk(kf[0], qt);
    float m = s[0];
#pragma unroll
    for (int i = 1; i < 16; ++i) m = __builtin_fmaxf(m, s[i]);
    m = xhalf_max(m);
    if (h == 1) q1[qt][1] = pack2(-m, 0.f);
  }
  f32x16 sA = qk(kf[0], 0), sB;
  u32x4 pA[2], pB[2];
  pB[0] = (u32x4){0u, 0u, 0u, 0u}; pB[1] = pB[0];
  vf[1].v0 = __builtin_bit_cast(bf16x8, pB[0]); vf[1].v1 = vf[1].v0;
  auto expack = [&](const f32x16& s, u32x4 (&p)[2]) {
#pragma unroll
    for (int i = 0; i < 8; ++i)
      p[i >> 2][i & 3] = pack2(__builtin_amdgcn_exp2f(s[2 * i]), __builtin_amdgcn_exp2f(s[2 * i + 1]));
  };
  auto tile = [&](KFrag& kc, KFrag& kn, VFrag& vp, VFrag& vc, const char* tcur, const char* tnext, bool dma, int pc,
                  int pbuf, int pi) {
    {
      rdk(kn, tnext);
      rdv(vc, tcur);
      f32x16 s = MFMA32(kc.k0, q0[1], zero);
      o[1] = MFMA32(vp.v0, __builtin_bit_cast(bf16x8, pB[0]), o[1]);
      sB = MFMA32(__builtin_bit_cast(bf16x8, kc.k1), __builtin_bit_cast(bf16x8, q1[1]), s);
      o[1] = MFMA32(vp.v1, __builtin_bit_cast(bf16x8, pB[1]), o[1]);
      expack(sA, pA);
#pragma unroll
      for (int i = 0; i < 4; ++i) {
        __builtin_amdgcn_sched_group_barrier(0x008, 1, 0);
        if (i < 2) __builtin_amdgcn_sched_group_barrier(0x100, 2, 0);
        __builtin_amdgcn_sched_group_barrier(0x400, 4, 0);
        __builtin_amdgcn_sched_group_barrier(0x002, 2, 0);
      }
      asm volatile("" : "+v"(pA[0]), "+v"(pA[1]));
    }
    SCHED_FENCE();
    {
      f32x16 s = MFMA32(kn.k0, q0[0], zero);
      o[0] = MFMA32(vc.v0, __builtin_bit_cast(bf16x8, pA[0]), o[0]);
      sA = MFMA32(__builtin_bit_cast(bf16x8, kn.k1), __builtin_bit_cast(bf16x8, q1[0]), s);
      o[0] = MFMA32(vc.v1, __builtin_bit_cast(bf16x8, pA[1]), o[0]);
      expack(sB, pB);
#pragma unroll
      for (int i = 0; i < 4; ++i) {
        __builtin_amdgcn_sched_group_barrier(0x008, 1, 0);
        __builtin_amdgcn_sched_group_barrier(0x400, 4, 0);
        __builtin_amdgcn_sched_group_barrier(0x002, 2, 0);
      }
      asm volatile("" : "+v"(pB[0]), "+v"(pB[1]));
    }
    SCHED_FENCE();
    if (dma) piece(pc, pbuf, pi);
    SCHED_FENCE();
  };
  int buf = 0;
#pragma unroll 1
  for (int c = 0; c < 8; ++c) {
    const int nbuf = buf == 2 ? 0 : buf + 1, nnbuf = nbuf == 2 ? 0 : nbuf + 1;
    const char* sb = smem + buf * 20480;
    const bool pre = c < 7;
    tile(kf[0], kf[1], vf[1], vf[0], sb, sb + 2560, pre, c + 1, nbuf, 1);
    tile(kf[1], kf[0], vf[0], vf[1], sb + 2560, sb + 5120, pre, c + 1, nbuf, 2);
    tile(kf[0], kf[1], vf[1], vf[0], sb + 5120, sb + 7680, pre, c + 1, nbuf, 3);
    tile(kf[1], kf[0], vf[0], vf[1], sb + 7680, sb + 10240, pre, c + 1, nbuf, 4);
    tile(kf[0], kf[1], vf[1], vf[0], sb + 10240, sb + 12800, false, 0, 0, 0);
    tile(kf[1], kf[0], vf[0], vf[1], sb + 12800, sb + 15360, false, 0, 0, 0);
    tile(kf[0], kf[1], vf[1], vf[0], sb + 15360, sb + 17920, false, 0, 0, 0);
    if (pre) { WAIT_VM0(); BARRIER(); }
    tile(kf[1], kf[0], vf[0], vf[1], sb + 17920, smem + nbuf * 20480, c < 6, c + 2, nnbuf, 0);
    buf = nbuf;
  }
  o[1] = MFMA32(vf[1].v0, __builtin_bit_cast(bf16x8, pB[0]), o[1]);
  o[1] = MFMA32(vf[1].v1, __builtin_bit_cast(bf16x8, pB[1]), o[1]);
}

DI void k1_attn(char* smem, int id, const u16* __restrict__ wq, const u16* __restrict__ w2,
                const u16* __restrict__ wo, char* __restrict__ ws) {
  const int tid = TID, l = tid & 63;
  const int w = __builtin_amdgcn_readfirstlane(tid >> 6);
  const int r = l & 31, h = l >> 5;
  const int tb = (id & 7) * 4 + ((id >> 3) & 3), hg = id >> 5;
  const int head = hg * 4 + w, tt0 = tb * 2;
  bf16x8 q0[2];
  u32x4 q1[2];
  __builtin_amdgcn_s_setprio(3);
  {
    const int sub = (l & 3) == 2 ? 20 : (l & 1) * 16;
    const int row0 = w * 4 + (l >> 4);
    const unsigned qo = row0 * 1152 + ((((l >> 2) & 3) - row0) & 3) * 36 + sub;
    const char* qsrc = (const char*)wq + hg * 144;
    const char* ysrc = ws + WS_YF + ((size_t)tt0 * 36 + w) * 1024;
    const unsigned lb = lds_addr(smem) + w * 1024, loff = l * 16;
    auto issue = [&](int st, int slot) {
      const unsigned d = lb + slot * 24576;
#pragma unroll
      for (int i = 0; i < 4; ++i) glds16s(qo, qsrc + (size_t)(st * 64 + i * 16) * 1152, d + i * 4096);
      glds16s(loff, ysrc + st * 4096, d + 16384);
      glds16s(loff, ysrc + st * 4096 + 36 * 1024, d + 20480);
    };
    issue(0, 0); issue(1, 1);
    const int g = l >> 4, q = (l & 15) >> 2, p = l & 3;
    const int aq = (4 * h + q) * 256 + ((w + q) & 3) * 64 + (g & 1) * 32 + p * 8;
    f32x16 qa[2];
#pragma unroll
    for (int i = 0; i < 16; ++i) { qa[0][i] = 0.f; qa[1][i] = 0.f; }
    int slot = 0;
#pragma unroll 1
    for (int st = 0; st < 9; ++st) {
      if (st < 8) WAIT_VM(6); else WAIT_VM(0);
      BARRIER();
      const char* sb = smem + slot * 24576;
      bf16x8 wf[4], yf[2][4];
#pragma unroll
      for (int ks = 0; ks < 4; ++ks) {
        wf[ks] = tr_frag(sb + aq + ks * 4096, sb + aq + ks * 4096 + 2048);
        yf[0][ks] = lds_frag(sb + 16384 + ks * 1024 + loff);
        yf[1][ks] = lds_frag(sb + 20480 + ks * 1024 + loff);
      }
      SCHED_FENCE();
      if (st < 7) issue(st + 2, slot == 0 ? 2 : slot - 1);
      SCHED_FENCE();
#pragma unroll
      for (int ks = 0; ks < 4; ++ks) { qa[0] = MFMA32(wf[ks], yf[0][ks], qa[0]); qa[1] = MFMA32(wf[ks], yf[1][ks], qa[1]); }
      slot = slot == 2 ? 0 : slot + 1;
    }
    const float* rinv = (const float*)(ws + WS_RINV);
#pragma unroll
    for (int qt = 0; qt < 2; ++qt) {
      const float a = rinv[(tt0 + qt) * 32 + r] * (1.4426950408889634f * 0.23570226039551584f);
      const u32x4 u = {pack2(qa[qt][0] * a, qa[qt][1] * a), pack2(qa[qt][2] * a, qa[qt][3] * a),
                       pack2(qa[qt][4] * a, qa[qt][5] * a), pack2(qa[qt][6] * a, qa[qt][7] * a)};
      q0[qt] = __builtin_bit_cast(bf16x8, u);
      q1[qt] = (u32x4){h == 1 ? pack2(qa[qt][10] * a, qa[qt][11] * a) : 0u, 0u, 0u, 0u};
    }
    syncthreads();
  }
  __builtin_amdgcn_s_setprio(0);
  f32x16 o[2];
  const char* kvimg = ws + WS_KV;
  attn_loop_fast(smem, kvimg, w, l, q0, q1, o);
  bool bad = false;
#pragma unroll
  for (int qt = 0; qt < 2; ++qt) {
    bad |= !(o[qt][10] <= 1.8446744e19f);
#pragma unroll
    for (int i = 0; i < 10; ++i) bad |= !(__builtin_fabsf(o[qt][i]) <= 3.0e38f);
  }
  volatile LDSP unsigned* flag = (volatile LDSP unsigned*)(smem + 73728 + 2048);
  if (tid == 0) *flag = 0u;
  syncthreads();
  if (bad) *flag = 1u;
  syncthreads();
  if (__builtin_amdgcn_readfirstlane(*flag) != 0u) {
    q1[0][1] = 0u; q1[1][1] = 0u;
    attn_loop_safe(smem, kvimg, w, l, q0, q1, o);
  }
  const auto rs = __builtin_amdgcn_make_buffer_rsrc(ws + WS_OF, 0, 64 * 36 * 1024, 0x00020000);
#pragma unroll
  for (int qt = 0; qt < 2; ++qt) {
    const float inv = 1.0f / o[qt][10];
    const unsigned dst = (tt0 + qt) * 36 * 1024;
    u32x4 u = {pack2(o[qt][0] * inv, o[qt][1] * inv), pack2(o[qt][2] * inv, o[qt][3] * inv),
               pack2(o[qt][4] * inv, o[qt][5] * inv), pack2(o[qt][6] * inv, o[qt][7] * inv)};
    __builtin_amdgcn_raw_buffer_store_b128(u, rs, dst + head * 1024 + l * 16, 0, 16);
    if (h == 0)
      __builtin_amdgcn_raw_buffer_store_b32(pack2(o[qt][8] * inv, o[qt][9] * inv), rs,
          dst + (32 + (head >> 3)) * 1024 + (((head >> 2) & 1) * 32 + r) * 16 + (head & 3) * 4, 0, 16);
  }
  signal(ws, CNT_O + (tb >> 1));
}

DI void k1_mlp(char* smem, int id, const u16* __restrict__ w1, const u16* __restrict__ w2,
               const u16* __restrict__ wo, const u16* __restrict__ x, const u16* __restrict__ b2,
               char* __restrict__ ws, u16* __restrict__ out) {
  const int tid = TID, l = tid & 63;
  const int w = __builtin_amdgcn_readfirstlane(tid >> 6);
  const int xcd = id & 7, j = id >> 3, rh = xcd >> 2, sq = xcd & 3;
  const int rb = rh * 8 + (j & 7), cb = sq * 4 + (j >> 3), nh = cb & 1;
  const int ttile = rb * 4 + w;
  const int h = l >> 5;
  const bool five = w < 2;
  unsigned woff1[5], woff2[5];
#pragma unroll
  for (int jj = 0; jj < 5; ++jj) {
    const int idx = (w + 4 * jj < 17 ? w + 4 * jj : 17) * 64 + l;
    const unsigned prow = idx / 36, pcol = (idx - prow * 36) * 16;
    woff1[jj] = prow * 9216 + pcol; woff2[jj] = prow * 1152 + pcol;
  }
  const char* w1src = (const char*)w1 + cb * 576;
  const char* w2src = (const char*)w2 + nh * 576;
  const char* ysrc = ws + WS_YF + (size_t)ttile * 36 * 1024;
  const char* hsrc = ws + WS_HF + (size_t)ttile * 288 * 1024;
  const unsigned loff = l * 16;
  const unsigned lbase = lds_addr(smem) + w * 1024;
  auto wpiece = [&](const char* g, unsigned dst, int jj) {
    if (jj < 4 || five) glds16s(woff1[jj], g, dst + jj * 4096);
  };
  auto bload = [&](const char* p, u32x4 (&d)[2]) {
    asm volatile("global_load_dwordx4 %0, %2, %3 sc1\n\tglobal_load_dwordx4 %1, %2, %3 offset:1024 sc1"
                 : "=&v"(d[0]), "=&v"(d[1]) : "v"(loff), "s"(p) : "memory");
  };
  const char* wp = w1src + (size_t)3 * 32 * 9216;
  const char* bp = ysrc + 2 * 2048;
  unsigned wstride = 32 * 9216, bstride = 2048;
  const int g = l >> 4, q = (l & 15) >> 2, p = l & 3;
  const int aoff = (4 * h + q) * 576 + (g & 1) * 32 + p * 8;
  f32x16 acc[9];
  bf16x8 fa[5], fb[4];
  u32x4 br[3][2];
  auto rd = [&](const char* sb, int m) { return tr_frag(sb + m * 64, sb + m * 64 + 8 * 576); };
  auto touch_a = [&]() { asm volatile("" : "+v"(fa[0]), "+v"(fa[1]), "+v"(fa[2]), "+v"(fa[3]), "+v"(fa[4])); };
  auto touch_b = [&]() { asm volatile("" : "+v"(fb[0]), "+v"(fb[1]), "+v"(fb[2]), "+v"(fb[3])); };
  auto group_a = [&](const u32x4& b, const char* sb, auto job0, auto job1) {
    const bf16x8 bb = __builtin_bit_cast(bf16x8, b);
    touch_a();
    SCHED_FENCE();
    acc[0] = MFMA32(fa[0], bb, acc[0]); SCHED_FENCE();
    fb[0] = rd(sb, 5); fb[1] = rd(sb, 6); SCHED_FENCE();
    acc[1] = MFMA32(fa[1], bb, acc[1]); SCHED_FENCE();
    fb[2] = rd(sb, 7); fb[3] = rd(sb, 8); SCHED_FENCE();
    acc[2] = MFMA32(fa[2], bb, acc[2]); SCHED_FENCE();
    job0(); SCHED_FENCE();
    acc[3] = MFMA32(fa[3], bb, acc[3]); SCHED_FENCE();
    job1(); SCHED_FENCE();
    acc[4] = MFMA32(fa[4], bb, acc[4]); SCHED_FENCE();
  };
  auto group_b = [&](const u32x4& b, const char* sb, auto job0, auto job1) {
    const bf16x8 bb = __builtin_bit_cast(bf16x8, b);
    SCHED_FENCE();
    acc[5] = MFMA32(fb[0], bb, acc[5]); SCHED_FENCE();
    fa[0] = rd(sb, 0); fa[1] = rd(sb, 1); fa[2] = rd(sb, 2); SCHED_FENCE();
    acc[6] = MFMA32(fb[1], bb, acc[6]); SCHED_FENCE();
    fa[3] = rd(sb, 3); fa[4] = rd(sb, 4); SCHED_FENCE();
    acc[7] = MFMA32(fb[2], bb, acc[7]); SCHED_FENCE();
    job0(); SCHED_FENCE();
    acc[8] = MFMA32(fb[3], bb, acc[8]); SCHED_FENCE();
    job1(); SCHED_FENCE();
  };
  auto nojob = []() {};
  auto step = [&](int ph, int i, auto cc) {
    constexpr int C = decltype(cc)::value;
    const int n = ph * 18 + i;
    if (C == 1 && n == 25) {
      GAS unsigned* f = (GAS unsigned*)(ws + WS_FLG) + ((rb * 16 + (cb ^ 1)) * 4 + w) * 16;
      for (unsigned spins = 0; spins < (1u << 22); ++spins) {
        if (__builtin_amdgcn_readfirstlane(__hip_atomic_load(f, __ATOMIC_RELAXED, __HIP_MEMORY_SCOPE_AGENT)) != 0u) break;
        __builtin_amdgcn_s_sleep(2);
      }
    }
    const unsigned wd = lbase + ((n + 3) & 3) * 18432;
    const char* sb = smem + (n & 3) * 18432 + aoff;
    const char* sn = smem + ((n + 1) & 3) * 18432 + aoff;
    group_a(br[C][0], sb, [&]() { bload(bp, br[(C + 2) % 3]); }, [&]() { wpiece(wp, wd, 0); });
    touch_b();
    group_b(br[C][0], sb + 16 * 576, [&]() { wpiece(wp, wd, 1); }, [&]() { wpiece(wp, wd, 2); });
    group_a(br[C][1], sb + 16 * 576, [&]() { wpiece(wp, wd, 3); }, [&]() { wpiece(wp, wd, 4); });
    touch_b();
    if (five) WAIT_VM(12); else WAIT_VM(10);
    asm volatile("" : "+v"(br[(C + 1) % 3][0]), "+v"(br[(C + 1) % 3][1]));
    BARRIER();
    group_b(br[C][1], sn, [&]() {
      if (C == 2 && n == 32) wstride = 0;
      wp += wstride;
      if (C == 2 && n == 14) {
        wp = w2src + (size_t)cb * 288 * 1152; wstride = 32 * 1152;
#pragma unroll
        for (int jj = 0; jj < 5; ++jj) woff1[jj] = woff2[jj];
      }
      if (C == 2 && n == 23) wp = w2src + (size_t)(cb ^ 1) * 288 * 1152;
      if (C == 0 && i == 15) bstride = 0;
      bp += bstride;
      if (C == 0 && n == 24) bp = hsrc + (size_t)(cb ^ 1) * 18 * 1024;
    }, nojob);
  };
#define PRIO 3
  if (PRIO & 1) __builtin_amdgcn_s_setprio(1);
  if (w == 0) glds16s(loff, ws + WS_B1F + cb * 1152, lds_addr(smem) + 73728);
  if (w == 1 && l < 8) glds16s(loff, ws + WS_B1F + cb * 1152 + 1024, lds_addr(smem) + 73728 + 1024);
#pragma unroll
  for (int st = 0; st < 3; ++st) {
#pragma unroll
    for (int jj = 0; jj < 5; ++jj) wpiece(w1src + (size_t)st * 32 * 9216, lbase + st * 18432, jj);
    if (st < 2) bload(ysrc + st * 2048, br[st]);
  }
  if (five) WAIT_VM(12); else WAIT_VM(10);
  BARRIER();
#pragma unroll
  for (int m = 0; m < 9; ++m)
#pragma unroll
    for (int gq = 0; gq < 4; ++gq) {
      const f32x4 bv = *(const f32x4*)(smem + 73728 + (m * 32 + 8 * gq + 4 * h) * 4);
#pragma unroll
      for (int e = 0; e < 4; ++e) acc[m][4 * gq + e] = bv[e];
    }
  const auto rp = __builtin_amdgcn_make_buffer_rsrc(ws + WS_PART, 0, 64 * 18 * 16 * 1024, 0x00020000);
  int nph = 2;
  asm volatile("" : "+s"(nph));
#pragma unroll 1
  for (int ph = 0; ph < nph; ++ph) {
    asm volatile("" : "+v"(br[0][0]), "+v"(br[0][1]));
    {
      const char* s0 = smem + ph * 2 * 18432 + aoff;
#pragma unroll
      for (int m = 0; m < 5; ++m) fa[m] = rd(s0, m);
    }
#pragma unroll 1
    for (int i = 0; i < 18; i += 3) { step(ph, i, IC(0)); step(ph, i + 1, IC(1)); step(ph, i + 2, IC(2)); }
    if (ph == 0) {
      const float k0 = -2.0f * 0.7978845608028654f * 1.4426950408889634f, k1 = k0 * 0.044715f;
      const auto rs = __builtin_amdgcn_make_buffer_rsrc(ws + WS_HF, 0, 64 * 288 * 1024, 0x00020000);
      const unsigned hbase = (ttile * 288 + cb * 18) * 1024 + l * 16;
#pragma unroll
      for (int m = 0; m < 9; ++m) {
#pragma unroll
        for (int s = 0; s < 2; ++s) {
          u32x4 u;
#pragma unroll
          for (int e = 0; e < 4; ++e)
            u[e] = pack2(gelu_tanh(acc[m][8 * s + 2 * e], k0, k1), gelu_tanh(acc[m][8 * s + 2 * e + 1], k0, k1));
          __builtin_amdgcn_raw_buffer_store_b128(u, rs, hbase + (m * 2 + s) * 1024, 0, 16);
        }
      }
      WAIT_VM0();
      asm volatile("" :: "v"(br[0][0]), "v"(br[0][1]), "v"(br[1][0]), "v"(br[1][1]));
      if (l == 0) __hip_atomic_store((GAS unsigned*)(ws + WS_FLG) + ((rb * 16 + cb) * 4 + w) * 16, 1u, __ATOMIC_RELAXED, __HIP_MEMORY_SCOPE_AGENT);
      if (PRIO & 2) __builtin_amdgcn_s_setprio(1); else __builtin_amdgcn_s_setprio(0);
      bp = hsrc + (size_t)cb * 18 * 1024;
      bload(bp, br[0]); bload(bp + 2048, br[1]);
      bp += 4096; bstride = 2048;
#pragma unroll
      for (int m = 0; m < 9; ++m)
#pragma unroll
        for (int i = 0; i < 16; ++i) acc[m][i] = 0.f;
      WAIT_VM0();
      BARRIER();
    }
  }
  const bool fin = j < 18;
  const int rbf = rh * 8 + (sq * 18 + j) / 9, nb = (sq * 18 + j) % 9, ft = rbf * 4 + w;
  const size_t obase = (size_t)(ft * 32 + (l & 31)) * DM + nb * 64 + 4 * h;
  WAIT_VM0();
  asm volatile("" :: "v"(br[0][0]), "v"(br[0][1]), "v"(br[1][0]), "v"(br[1][1]));
  BARRIER();
#pragma unroll
  for (int m = 0; m < 9; ++m)
#pragma unroll
    for (int s = 0; s < 2; ++s) {
      const u32x4 u = {pack2(acc[m][8 * s], acc[m][8 * s + 1]), pack2(acc[m][8 * s + 2], acc[m][8 * s + 3]),
                       pack2(acc[m][8 * s + 4], acc[m][8 * s + 5]), pack2(acc[m][8 * s + 6], acc[m][8 * s + 7])};
      __builtin_amdgcn_raw_buffer_store_b128(u, rp, (((ttile * 18 + nh * 9 + m) * 2 + s) * 8 + (cb >> 1)) * 1024 + l * 16, 0, 16);
    }
  if (!fin) { signal(ws, CNT_P + rb); return; }
#pragma unroll 1
  for (int i = 0; i < 18; ++i) {
    const int pc = w + 4 * i, kp = pc * 8 + (l >> 3);
    const int rr = (kp - 512) >> 4, ii = kp & 15, hh = (ii >> 2) & 1, jj = 4 * (ii >> 3) + (ii & 3);
    const int row = i < 16 ? (kp >> 4) * 18 + ii : 18 * (8 * rr + 4 * hh + (jj >> 1)) + 16 + (jj & 1);
    const int ch = (l & 7) ^ (((kp >> 1) & 1) << 2);
    glds16s(row * 1152 + ch * 16, (const char*)wo + nb * 128, lds_addr(smem) + pc * 1024);
  }
  WAIT_VM(18);
  BARRIER();
  if (tid == 0) __hip_atomic_fetch_add((GAS unsigned*)(ws + WS_CNT) + (CNT_P + rb) * 64, 1u, __ATOMIC_RELAXED, __HIP_MEMORY_SCOPE_AGENT);
  __builtin_amdgcn_s_setprio(0);
  u32x2 xv[2][4], bv[2][4];
#pragma unroll
  for (int t = 0; t < 2; ++t)
#pragma unroll
    for (int gq = 0; gq < 4; ++gq) {
      xv[t][gq] = *(const u32x2*)(x + obase + t * 32 + 8 * gq);
      bv[t][gq] = *(const u32x2*)(b2 + nb * 64 + t * 32 + 4 * h + 8 * gq);
    }
  f32x16 oa[2];
#pragma unroll
  for (int t = 0; t < 2; ++t)
#pragma unroll
    for (int gq = 0; gq < 4; ++gq) {
      oa[t][4 * gq + 0] = bflo(xv[t][gq][0]) + bflo(bv[t][gq][0]);
      oa[t][4 * gq + 1] = bfhi(xv[t][gq][0]) + bfhi(bv[t][gq][0]);
      oa[t][4 * gq + 2] = bflo(xv[t][gq][1]) + bflo(bv[t][gq][1]);
      oa[t][4 * gq + 3] = bfhi(xv[t][gq][1]) + bfhi(bv[t][gq][1]);
    }
  await(ws, CNT_P + rbf, 16u);
  {
    u32x4 pv[2][2][8];
#pragma unroll
    for (int t = 0; t < 2; ++t)
#pragma unroll
      for (int s = 0; s < 2; ++s)
#pragma unroll
        for (int src = 0; src < 8; ++src)
          pv[t][s][src] = __builtin_amdgcn_raw_buffer_load_b128(rp, (((ft * 18 + nb * 2 + t) * 2 + s) * 8 + src) * 1024 + l * 16, 0, 16);
#pragma unroll
    for (int t = 0; t < 2; ++t)
#pragma unroll
      for (int s = 0; s < 2; ++s)
#pragma unroll
        for (int src = 0; src < 8; ++src)
#pragma unroll
          for (int e = 0; e < 4; ++e) {
            const unsigned u = pv[t][s][src][e];
            oa[t][8 * s + 2 * e] += bflo(u);
            oa[t][8 * s + 2 * e + 1] += bfhi(u);
          }
  }
  asm volatile("" : "+v"(oa[0]), "+v"(oa[1]));
  WAIT_VM0();
  await(ws, CNT_O + rbf, 16u);
  {
    const auto ro = __builtin_amdgcn_make_buffer_rsrc(ws + WS_OF, 0, 64 * 36 * 1024, 0x00020000);
    u32x4 of[36];
#pragma unroll
    for (int ks = 0; ks < 36; ++ks) of[ks] = __builtin_amdgcn_raw_buffer_load_b128(ro, (ft * 36 + ks) * 1024 + l * 16, 0, 16);
    const int ao = (4 * h + q) * 128 + (g & 1) * 32 + p * 8, sw = ((q >> 1) & 1) * 64;
    const char* a0 = smem + ao + sw;
    const char* a1 = smem + ao + (64 ^ sw);
    bf16x8 af[2][3][2];
    auto rdw = [&](int b, int kb) {
#pragma unroll
      for (int i = 0; i < 3; ++i) {
        const int ks = kb * 3 + i;
        af[b][i][0] = tr_frag(a0 + ks * 2048, a0 + ks * 2048 + 1024);
        af[b][i][1] = tr_frag(a1 + ks * 2048, a1 + ks * 2048 + 1024);
      }
    };
    rdw(0, 0);
#pragma unroll
    for (int kb = 0; kb < 12; ++kb) {
      const int b = kb & 1;
      asm volatile("" : "+v"(af[b][0][0]), "+v"(af[b][0][1]), "+v"(af[b][1][0]), "+v"(af[b][1][1]), "+v"(af[b][2][0]), "+v"(af[b][2][1]));
      SCHED_FENCE();
      if (kb + 1 < 12) rdw(b ^ 1, kb + 1);
      SCHED_FENCE();
#pragma unroll
      for (int i = 0; i < 3; ++i) {
        oa[0] = MFMA32(af[b][i][0], __builtin_bit_cast(bf16x8, of[kb * 3 + i]), oa[0]);
        oa[1] = MFMA32(af[b][i][1], __builtin_bit_cast(bf16x8, of[kb * 3 + i]), oa[1]);
      }
      SCHED_FENCE();
    }
  }
#pragma unroll
  for (int t = 0; t < 2; ++t)
#pragma unroll
    for (int gq = 0; gq < 4; ++gq) {
      const u32x2 u = {pack2(oa[t][4 * gq], oa[t][4 * gq + 1]), pack2(oa[t][4 * gq + 2], oa[t][4 * gq + 3])};
      *(u32x2*)(out + obase + t * 32 + 8 * gq) = u;
    }
}

__global__ __launch_bounds__(256, 2) void k1_kernel(const u16* __restrict__ wq,
                                                    const u16* __restrict__ w1, const u16* __restrict__ w2,
                                                    const u16* __restrict__ wo, const u16* __restrict__ x,
                                                    const u16* __restrict__ b2, char* __restrict__ ws,
                                                    u16* __restrict__ out) {
  extern __shared__ __attribute__((aligned(16))) char smem[];
  const int b = BID;
  if (b < 256) {
    const char* pc = (const char*)__builtin_amdgcn_s_getpc();
    unsigned d0, d1;
    const unsigned off = TID * 64;
    asm volatile("global_load_dword %0, %2, %3\n\tglobal_load_dword %1, %4, %3"
                 : "=&v"(d0), "=&v"(d1) : "v"(off), "s"(pc), "v"(off < K1_CODE - 16384 ? off + 16384 : off) : "memory");
    k1_attn(smem, b, wq, w2, wo, ws);
    asm volatile("" :: "v"(d0), "v"(d1));
  }
  else k1_mlp(smem, b - 256, w1, w2, wo, x, b2, ws, out);
}

#if defined(__HIP_DEVICE_COMPILE__)
#pragma clang attribute pop
#endif

extern "C" void kernel_launch(void* const* d_in, const int* in_sizes, int n_in, void* d_out, int out_size,
                              void* d_ws, size_t ws_size, hipStream_t stream) {
  const u16* x = (const u16*)d_in[0];
  const u16* wq = (const u16*)d_in[1];
  const u16* wk = (const u16*)d_in[2];
  const u16* wv = (const u16*)d_in[3];
  const u16* wo = (const u16*)d_in[4];
  const u16* w1 = (const u16*)d_in[6];
  const u16* b1 = (const u16*)d_in[7];
  const u16* w2 = (const u16*)d_in[8];
  const u16* b2 = (const u16*)d_in[9];
  char* ws = (char*)d_ws;
  (void)hipFuncSetAttribute((const void*)k1_kernel, hipFuncAttributeMaxDynamicSharedMemorySize, K1_LDS);
  (void)hipFuncSetAttribute((const void*)k0_kernel, hipFuncAttributeMaxDynamicSharedMemorySize, K0_LDS);
  k0_kernel<<<192, 512, K0_LDS, stream>>>(x, wk, wv, b1, w1, wq, ws);
  k1_kernel<<<512, 256, K1_LDS, stream>>>(wq, w1, w2, wo, x, b2, ws, (u16*)d_out);
}
```

```cpp
#include <hip/hip_runtime.h>
#include <hip/hip_bf16.h>
#include <stdint.h>
#include <type_traits>

#if defined(__HIP_DEVICE_COMPILE__)
#pragma clang attribute push(__attribute__((target("no-packed-fp32-ops"))), apply_to = function)
#endif

typedef short bf16x8 __attribute__((ext_vector_type(8)));
typedef short bf16x4 __attribute__((ext_vector_type(4)));
typedef __bf16 bfx2 __attribute__((ext_vector_type(2)));
typedef float f32x2 __attribute__((ext_vector_type(2)));
typedef float f32x4 __attribute__((ext_vector_type(4)));
typedef float f32x16 __attribute__((ext_vector_type(16)));
typedef unsigned u32x2 __attribute__((ext_vector_type(2)));
typedef unsigned u32x4 __attribute__((ext_vector_type(4)));
typedef unsigned u32x4_a8 __attribute__((ext_vector_type(4), aligned(8)));
typedef unsigned short u16;
#define LDSP __attribute__((address_space(3)))
#define GAS __attribute__((address_space(1)))
#define DI __device__ __forceinline__
#define MFMA32(a, b, c) __builtin_amdgcn_mfma_f32_32x32x16_bf16((a), (b), (c), 0, 0, 0)
#define IC(n) std::integral_constant<int, n>{}

constexpr int SEQ = 2048, DM = 576, FF = 4608;
constexpr float LN_EPS = 1e-5f;
constexpr size_t WS_CNT = 0;
constexpr int CNT_P = 0;
constexpr size_t WS_FLG = 16384;
constexpr size_t WS_OFLG = WS_FLG + 65536;
constexpr size_t WS_B1F = WS_OFLG + 16384;
constexpr size_t WS_RINV = WS_B1F + 18432;
constexpr size_t WS_HF = 131072;
constexpr size_t WS_KV = WS_HF + (size_t)64 * 288 * 1024;
constexpr size_t WS_OF = WS_KV + (size_t)64 * 2560;
constexpr size_t WS_YF = WS_OF + (size_t)64 * 36 * 1024;
constexpr size_t WS_PART = WS_YF + (size_t)64 * 36 * 1024;

DI unsigned pack2(float a, float b) {
  f32x2 p = {a, b};
  return __builtin_bit_cast(unsigned, __builtin_convertvector(p, bfx2));
}
DI float bflo(unsigned u) { return __builtin_bit_cast(float, u << 16); }
DI float bfhi(unsigned u) { return __builtin_bit_cast(float, u & 0xffff0000u); }
DI void glds16s(unsigned voff, const void* sbase, unsigned lds) {
  unsigned keep;
  asm volatile("s_mov_b32 %0, m0\n\ts_mov_b32 m0, %3\n\ts_nop 0\n\tglobal_load_lds_dwordx4 %1, %2\n\ts_mov_b32 m0, %0"
               : "=&s"(keep) : "v"(voff), "s"(sbase), "s"(lds) : "memory");
}
DI unsigned lds_addr(const char* p) { return (unsigned)(size_t)(LDSP const char*)p; }
DI bf16x8 tr_frag(const char* lds_a, const char* lds_b) {
  bf16x4 a = __builtin_amdgcn_ds_read_tr16_b64_v4i16((LDSP bf16x4*)lds_a);
  bf16x4 b = __builtin_amdgcn_ds_read_tr16_b64_v4i16((LDSP bf16x4*)lds_b);
  return __builtin_shufflevector(a, b, 0, 1, 2, 3, 4, 5, 6, 7);
}
DI bf16x8 lds_frag(const char* p) { return *(const bf16x8*)p; }
DI float xhalf(float v) {
  const int lane = __builtin_amdgcn_mbcnt_hi(~0u, __builtin_amdgcn_mbcnt_lo(~0u, 0u));
  return __builtin_bit_cast(float, __builtin_amdgcn_ds_bpermute((lane ^ 32) << 2, __builtin_bit_cast(int, v)));
}
DI float xhalf_sum(float v) { return v + xhalf(v); }
DI float xhalf_max(float v) { return __builtin_fmaxf(v, xhalf(v)); }
DI void syncthreads() {
  __builtin_amdgcn_fence(__ATOMIC_RELEASE, "workgroup");
  __builtin_amdgcn_s_barrier();
  __builtin_amdgcn_fence(__ATOMIC_ACQUIRE, "workgroup");
}
#define TID ((int)__builtin_amdgcn_workitem_id_x())
#define BID ((int)__builtin_amdgcn_workgroup_id_x())
#define WAIT_VM0() asm volatile("s_waitcnt vmcnt(0)" ::: "memory")
#define WAIT_VM(n) asm volatile("s_waitcnt vmcnt(" #n ")" ::: "memory")
#define BARRIER() __builtin_amdgcn_s_barrier()
#define SCHED_FENCE() __builtin_amdgcn_sched_barrier(0)
#define ONES2 0x3f803f80u

DI void signal(char* ws, int idx) {
  WAIT_VM0();
  syncthreads();
  if (TID == 0) __hip_atomic_fetch_add((GAS unsigned*)(ws + WS_CNT) + idx * 64, 1u, __ATOMIC_RELAXED, __HIP_MEMORY_SCOPE_AGENT);
}
DI void await(char* ws, int idx, unsigned want) {
  if (TID < 64) {
    GAS unsigned* c = (GAS unsigned*)(ws + WS_CNT) + idx * 64;
    for (unsigned spins = 0; spins < (1u << 22); ++spins) {
      if (__builtin_amdgcn_readfirstlane(__hip_atomic_load(c, __ATOMIC_RELAXED, __HIP_MEMORY_SCOPE_AGENT)) >= want) break;
      __builtin_amdgcn_s_sleep(8);
    }
  }
  syncthreads();
}
DI float gelu_tanh(float x, float k0, float k1) {
  const float z = x * __builtin_fmaf(x * x, k1, k0);
  return x * __builtin_amdgcn_rcpf(1.0f + __builtin_amdgcn_exp2f(z));
}

template <int CTRL> DI float dpp_f(float v) {
  return __builtin_bit_cast(float, __builtin_amdgcn_update_dpp(0, __builtin_bit_cast(int, v), CTRL, 0xf, 0xf, true));
}
DI float sum8(float v) {
  v += dpp_f<0xB1>(v);
  v += dpp_f<0x4E>(v);
  v += dpp_f<0x141>(v);
  return v;
}
DI float dot2(unsigned a, unsigned b, float c) {
  return __builtin_amdgcn_fdot2_f32_bf16(__builtin_bit_cast(bfx2, a), __builtin_bit_cast(bfx2, b), c, false);
}
DI void k0_ln(const u16* __restrict__ x, int tok0, char* __restrict__ ws) {
  const int l = TID & 63, c8 = l & 7, tok = tok0 + (l >> 3);
  u32x4 v[9];
#pragma unroll
  for (int i = 0; i < 9; ++i) v[i] = *(const u32x4*)((const char*)x + (size_t)tok * 1152 + (c8 + 8 * i) * 16);
  float f[72];
  float sx = 0.f;
#pragma unroll
  for (int i = 0; i < 9; ++i)
#pragma unroll
    for (int e = 0; e < 4; ++e) {
      f[8 * i + 2 * e] = bflo(v[i][e]); f[8 * i + 2 * e + 1] = bfhi(v[i][e]);
      sx += f[8 * i + 2 * e] + f[8 * i + 2 * e + 1];
    }
  const float m = sum8(sx) * (1.0f / DM);
  float sq = 0.f;
#pragma unroll
  for (int i = 0; i < 72; ++i) { f[i] -= m; sq = __builtin_fmaf(f[i], f[i], sq); }
  const float var = sum8(sq) * (1.0f / DM);
  const float r1 = 1.0f / __builtin_sqrtf(var + LN_EPS);
  const float s2 = __builtin_sqrtf(var * r1 * r1 + LN_EPS);
  const float a2 = r1 / s2;
  if (c8 == 0) ((float*)(ws + WS_RINV))[tok] = s2;
  const bool odd = c8 & 1;
  char* dst = ws + WS_YF + ((size_t)(tok >> 5) * 36 + (c8 >> 1)) * 1024 + ((c8 & 1) * 32 + (tok & 31)) * 16;
#pragma unroll
  for (int i = 0; i < 9; ++i) {
    unsigned u[4];
#pragma unroll
    for (int e = 0; e < 4; ++e) u[e] = pack2(f[8 * i + 2 * e] * a2, f[8 * i + 2 * e + 1] * a2);
    const unsigned s0 = odd ? u[0] : u[2], s1 = odd ? u[1] : u[3];
    const unsigned r0 = __builtin_amdgcn_update_dpp(0, (int)s0, 0xB1, 0xf, 0xf, true);
    const unsigned r1u = __builtin_amdgcn_update_dpp(0, (int)s1, 0xB1, 0xf, 0xf, true);
    const u32x4 o = odd ? (u32x4){r0, r1u, u[2], u[3]} : (u32x4){u[0], u[1], r0, r1u};
    *(u32x4*)(dst + i * 4096) = o;
  }
}

constexpr int K0_LDS = 36864 + 73728 + 1024;
DI void k0_kv(char* smem, const u16* __restrict__ x, const u16* __restrict__ wk, const u16* __restrict__ wv, int tt,
              char* __restrict__ ws) {
  const int tid = TID, l = tid & 63;
  const int w = __builtin_amdgcn_readfirstlane(tid >> 6);
  const int r = l & 31, h = l >> 5;
  const int kq = w >> 1, mat = w & 1;
  char* wimg = smem + 36864 + w * 9216;
  {
    const int sub = (l & 3) == 2 ? 20 : (l & 1) * 16;
    const char* wsrc = (const char*)(mat ? wv : wk) + kq * 144 * 36;
#pragma unroll
    for (int i = 0; i < 9; ++i) glds16s((l >> 2) * 36 + sub, wsrc + i * 16 * 36, lds_addr(wimg) + i * 1024);
    const int row = (w & 3) * 8 + (l >> 3), ch = (l & 7) ^ ((row >> 1) & 7);
    const char* xsrc = (const char*)x + (size_t)tt * 32 * 1152 + (w >> 2) * 128;
#pragma unroll
    for (int i = 0; i < 5; ++i)
      if (i < 4 || w < 4) glds16s(row * 1152 + ch * 16, xsrc + i * 256, lds_addr(smem) + (i * 8 + w) * 1024);
  }
  WAIT_VM0();
  BARRIER();
  const int g = l >> 4, q = (l & 15) >> 2, p = l & 3;
  const int aoff = (8 * (g >> 1) + q) * 64 + (g & 1) * 32 + p * 8;
  f32x16 acc;
#pragma unroll
  for (int i = 0; i < 16; ++i) acc[i] = 0.f;
  float sx = 0.f, sxx = 0.f, cs = 0.f;
  auto kloop = [&](auto matc) {
    constexpr int MAT = decltype(matc)::value;
#pragma unroll 1
    for (int k3 = 0; k3 < 9; k3 += 3) {
      bf16x8 wf[3], xf[3];
#pragma unroll
      for (int i = 0; i < 3; ++i) {
        const int kst = kq * 9 + k3 + i;
        const char* ap = wimg + aoff + (k3 + i) * 1024;
        wf[i] = tr_frag(ap, ap + 256);
        xf[i] = lds_frag(smem + (kst >> 2) * 4096 + r * 128 + (((2 * (kst & 3) + h) ^ ((r >> 1) & 7)) << 4));
      }
#pragma unroll
      for (int i = 0; i < 3; ++i) {
        if (MAT == 0) acc = MFMA32(wf[i], xf[i], acc); else acc = MFMA32(xf[i], wf[i], acc);
      }
#pragma unroll
      for (int i = 0; i < 3; ++i) {
        const u32x4 ux = __builtin_bit_cast(u32x4, xf[i]), uw = __builtin_bit_cast(u32x4, wf[i]);
#pragma unroll
        for (int e = 0; e < 4; ++e) { sx = dot2(ux[e], ONES2, sx); sxx = dot2(ux[e], ux[e], sxx); cs = dot2(uw[e], ONES2, cs); }
      }
      asm volatile("" : "+v"(sx), "+v"(sxx), "+v"(cs));
    }
  };
  if (mat == 0) kloop(IC(0)); else kloop(IC(1));
  {
    char* red = wimg + l * 16;
    if (kq != 0) {
#pragma unroll
      for (int gq = 0; gq < 4; ++gq) *(f32x4*)(red + gq * 1024) = (f32x4){acc[4 * gq], acc[4 * gq + 1], acc[4 * gq + 2], acc[4 * gq + 3]};
      *(f32x4*)(red + 4096) = (f32x4){sx, sxx, cs, 0.f};
    }
    syncthreads();
    if (kq != 0) return;
#pragma unroll
    for (int s = 1; s < 4; ++s) {
      const char* d = red + 2 * s * 9216;
#pragma unroll
      for (int gq = 0; gq < 4; ++gq) {
        const f32x4 v = *(const f32x4*)(d + gq * 1024);
#pragma unroll
        for (int e = 0; e < 4; ++e) acc[4 * gq + e] += v[e];
      }
      const f32x4 v = *(const f32x4*)(d + 4096);
      sx += v[0]; sxx += v[1]; cs += v[2];
    }
  }
  sx = xhalf_sum(sx); sxx = xhalf_sum(sxx); cs = xhalf_sum(cs);
  const float mean = sx * (1.0f / DM);
  const float var = __builtin_fmaxf(sxx * (1.0f / DM) - mean * mean, 0.0f);
  const float a1 = 1.0f / __builtin_sqrtf(var + LN_EPS), c1 = -mean * a1;
  float* scr = (float*)(smem + 36864 + 73728 + w * 256);
  char* img = ws + WS_KV + (size_t)tt * 2560;
  if (mat == 0) {
    if (h == 0) scr[r] = cs;
    __builtin_amdgcn_s_waitcnt(0xc07f);
    __builtin_amdgcn_wave_barrier();
    float v[12];
#pragma unroll
    for (int gg = 0; gg < 3; ++gg) {
      const f32x4 c4 = *(const f32x4*)&scr[8 * gg + 4 * h];
#pragma unroll
      for (int e = 0; e < 4; ++e) v[4 * gg + e] = __builtin_fmaf(a1, acc[4 * gg + e], c1 * c4[e]);
    }
    const u32x4 u = {pack2(v[0], v[1]), pack2(v[2], v[3]), pack2(v[4], v[5]), pack2(v[6], v[7])};
    *(u32x4*)(img + l * 16) = u;
    if (h == 1) *(u32x2*)(img + 1024 + r * 8) = (u32x2){pack2(v[10], v[11]), 0x00003f80u};
  } else {
    if (h == 0) { scr[r] = a1; scr[32 + r] = c1; }
    __builtin_amdgcn_s_waitcnt(0xc07f);
    __builtin_amdgcn_wave_barrier();
    float v[16];
#pragma unroll
    for (int gg = 0; gg < 4; ++gg) {
      const f32x4 a4 = *(const f32x4*)&scr[8 * gg + 4 * h];
      const f32x4 c4 = *(const f32x4*)&scr[32 + 8 * gg + 4 * h];
#pragma unroll
      for (int e = 0; e < 4; ++e) v[4 * gg + e] = __builtin_fmaf(a4[e], acc[4 * gg + e], c4[e] * cs);
    }
    const int vr = r >= 22 ? r - 6 : r;
    if (r < 16 || r == 18 || r == 19 || r == 22 || r == 23) {
#pragma unroll
      for (int s = 0; s < 2; ++s) {
        u32x4 u = {pack2(v[8 * s], v[8 * s + 1]), pack2(v[8 * s + 2], v[8 * s + 3]),
                   pack2(v[8 * s + 4], v[8 * s + 5]), pack2(v[8 * s + 6], v[8 * s + 7])};
        if (r == 18) u = (u32x4){ONES2, ONES2, ONES2, ONES2};
        if (r == 19) u = (u32x4){0u, 0u, 0u, 0u};
        *(u32x4*)(img + 1280 + ((s * 2 + h) * 20 + vr) * 16) = u;
      }
    }
  }
}

__global__ __launch_bounds__(512, 2) void k0_kernel(const u16* __restrict__ x, const u16* __restrict__ wk,
                                                    const u16* __restrict__ wv, const u16* __restrict__ b1,
                                                    char* __restrict__ ws) {
  extern __shared__ __attribute__((aligned(16))) char smem[];
  const int b = BID, tid = TID;
  if (b < 64) {
    k0_kv(smem, x, wk, wv, b, ws);
  } else {
    const int i = b - 64;
    if (tid < 128) k0_ln(x, i * 16 + (tid >> 6) * 8, ws);
    else if (tid < 164) ((float*)(ws + WS_B1F))[i * 36 + tid - 128] = bflo(b1[i * 36 + tid - 128]);
    else if (tid >= 192 && tid < 202) *(unsigned*)(ws + WS_FLG + (i * 10 + tid - 192) * 64) = 0u;
    else if (i == 0 && tid >= 256 && tid < 320) *(unsigned*)(ws + WS_CNT + (tid - 256) * 256) = 0u;
  }
}

constexpr int K1_LDS = 73728 + 2048 + 64;
constexpr unsigned K1_CODE = 20480;

struct KFrag { bf16x8 k0; u32x4 k1; };
struct VFrag { bf16x8 v0, v1; };

DI void attn_loop_safe(char* smem, const char* __restrict__ kvimg, int w, int l, const bf16x8 (&q0)[2],
                       const u32x4 (&q1)[2], f32x16 (&o)[2]) {
  const int r = l & 31, h = l >> 5;
  const int dv = (r == 22) ? 18 : (r >= 20 ? 19 : r);
  const int koff = l * 16, k1off = 1024 + r * 8, voff = 1280 + (h * 20 + dv) * 16;
  auto stage = [&](int c) {
#pragma unroll
    for (int i = 0; i < 5; ++i)
      glds16s(l * 16, kvimg + (size_t)c * 20480 + (5 * w + i) * 1024, lds_addr(smem) + (c & 1) * 20480 + (5 * w + i) * 1024);
  };
  float mrun[2] = {-3.0e38f, -3.0e38f};
#pragma unroll
  for (int qt = 0; qt < 2; ++qt)
#pragma unroll
    for (int i = 0; i < 16; ++i) o[qt][i] = 0.f;
  const f32x16 zero = {};
  stage(0);
#pragma unroll 1
  for (int c = 0; c < 8; ++c) {
    WAIT_VM0();
    BARRIER();
    if (c + 1 < 8) stage(c + 1);
    const char* sb = smem + (c & 1) * 20480;
#pragma unroll 1
    for (int t = 0; t < 8; ++t) {
      const char* tb = sb + t * 2560;
      const bf16x8 kf0 = lds_frag(tb + koff);
      const u32x2 k1 = *(const u32x2*)(tb + k1off);
      const u32x4 k1v = {k1[0], k1[1], 0u, 0u};
      const bf16x8 vf0 = lds_frag(tb + voff), vf1 = lds_frag(tb + voff + 640);
#pragma unroll
      for (int qt = 0; qt < 2; ++qt) {
        f32x16 s = MFMA32(kf0, q0[qt], zero);
        s = MFMA32(__builtin_bit_cast(bf16x8, k1v), __builtin_bit_cast(bf16x8, q1[qt]), s);
        float m = s[0];
#pragma unroll
        for (int i = 1; i < 16; ++i) m = __builtin_fmaxf(m, s[i]);
        m = xhalf_max(m);
        const float mn = __builtin_fmaxf(mrun[qt], m);
        const float alpha = __builtin_amdgcn_exp2f(mrun[qt] - mn);
        mrun[qt] = mn;
#pragma unroll
        for (int i = 0; i < 16; ++i) { o[qt][i] *= alpha; s[i] -= mn; }
        unsigned p[8];
#pragma unroll
        for (int i = 0; i < 8; ++i)
          p[i] = pack2(__builtin_amdgcn_exp2f(s[2 * i]), __builtin_amdgcn_exp2f(s[2 * i + 1]));
        const u32x4 pa = {p[0], p[1], p[2], p[3]}, pb = {p[4], p[5], p[6], p[7]};
        o[qt] = MFMA32(vf0, __builtin_bit_cast(bf16x8, pa), o[qt]);
        o[qt] = MFMA32(vf1, __builtin_bit_cast(bf16x8, pb), o[qt]);
      }
    }
  }
}

DI void attn_loop_fast(char* smem, const char* __restrict__ kvimg, int w, int l, const bf16x8 (&q0)[2],
                       u32x4 (&q1)[2], f32x16 (&o)[2]) {
  const int r = l & 31, h = l >> 5;
  const int dv = (r == 22) ? 18 : (r >= 20 ? 19 : r);
  const int koff = l * 16, k1off = 1024 + r * 8, voff = 1280 + (h * 20 + dv) * 16;
  const char* gsrc = kvimg + w * 1024;
  const unsigned lbase = lds_addr(smem) + w * 1024, loff = l * 16;
  const bool three = w < 2;
  auto piece = [&](int c, int buf, int i) {
    if (i < 2 || three) glds16s(loff, gsrc + (size_t)(c < 15 ? c : 15) * 10240 + i * 4096, lbase + buf * 10240 + i * 4096);
  };
  const f32x16 zero = {};
  auto rdk = [&](KFrag& k, const char* tb) {
    k.k0 = lds_frag(tb + koff);
    k.k1 = *(const u32x4_a8*)(tb + k1off);
  };
  auto rdv = [&](VFrag& v, const char* tb) { v.v0 = lds_frag(tb + voff); v.v1 = lds_frag(tb + voff + 640); };
  auto qk = [&](const KFrag& k, int qt) {
    f32x16 s = MFMA32(k.k0, q0[qt], zero);
    return MFMA32(__builtin_bit_cast(bf16x8, k.k1), __builtin_bit_cast(bf16x8, q1[qt]), s);
  };
#pragma unroll
  for (int qt = 0; qt < 2; ++qt)
#pragma unroll
    for (int i = 0; i < 16; ++i) o[qt][i] = 0.f;
  KFrag kf[2];
  VFrag vf[2];
  rdk(kf[0], smem + 6 * 10240);
#pragma unroll 1
  for (int c = 1; c < 4; ++c) { piece(c, c - 1, 0); piece(c, c - 1, 1); piece(c, c - 1, 2); }
#pragma unroll
  for (int qt = 0; qt < 2; ++qt) {
    const f32x16 s = qk(kf[0], qt);
    float m = s[0];
#pragma unroll
    for (int i = 1; i < 16; ++i) m = __builtin_fmaxf(m, s[i]);
    m = xhalf_max(m);
    if (h == 1) q1[qt][1] = pack2(-m, 0.f);
  }
  f32x16 sA = qk(kf[0], 0), sB;
  u32x4 pA[2], pB[2];
  pB[0] = (u32x4){0u, 0u, 0u, 0u}; pB[1] = pB[0];
  vf[1].v0 = __builtin_bit_cast(bf16x8, pB[0]); vf[1].v1 = vf[1].v0;
  auto expack = [&](const f32x16& s, u32x4 (&p)[2]) {
#pragma unroll
    for (int i = 0; i < 8; ++i)
      p[i >> 2][i & 3] = pack2(__builtin_amdgcn_exp2f(s[2 * i]), __builtin_amdgcn_exp2f(s[2 * i + 1]));
  };
  auto tile = [&](KFrag& kc, KFrag& kn, VFrag& vp, VFrag& vc, const char* tcur, const char* tnext, bool dma, int pc,
                  int pbuf, int pi) {
    {
      rdk(kn, tnext);
      rdv(vc, tcur);
      f32x16 s = MFMA32(kc.k0, q0[1], zero);
      o[1] = MFMA32(vp.v0, __builtin_bit_cast(bf16x8, pB[0]), o[1]);
      sB = MFMA32(__builtin_bit_cast(bf16x8, kc.k1), __builtin_bit_cast(bf16x8, q1[1]), s);
      o[1] = MFMA32(vp.v1, __builtin_bit_cast(bf16x8, pB[1]), o[1]);
      expack(sA, pA);
#pragma unroll
      for (int i = 0; i < 4; ++i) {
        __builtin_amdgcn_sched_group_barrier(0x008, 1, 0);
        if (i < 2) __builtin_amdgcn_sched_group_barrier(0x100, 2, 0);
        __builtin_amdgcn_sched_group_barrier(0x400, 4, 0);
        __builtin_amdgcn_sched_group_barrier(0x002, 2, 0);
      }
      asm volatile("" : "+v"(pA[0]), "+v"(pA[1]));
    }
    SCHED_FENCE();
    {
      f32x16 s = MFMA32(kn.k0, q0[0], zero);
      o[0] = MFMA32(vc.v0, __builtin_bit_cast(bf16x8, pA[0]), o[0]);
      sA = MFMA32(__builtin_bit_cast(bf16x8, kn.k1), __builtin_bit_cast(bf16x8, q1[0]), s);
      o[0] = MFMA32(vc.v1, __builtin_bit_cast(bf16x8, pA[1]), o[0]);
      expack(sB, pB);
#pragma unroll
      for (int i = 0; i < 4; ++i) {
        __builtin_amdgcn_sched_group_barrier(0x008, 1, 0);
        __builtin_amdgcn_sched_group_barrier(0x400, 4, 0);
        __builtin_amdgcn_sched_group_barrier(0x002, 2, 0);
      }
      asm volatile("" : "+v"(pB[0]), "+v"(pB[1]));
    }
    SCHED_FENCE();
    if (dma) piece(pc, pbuf, pi);
    SCHED_FENCE();
  };
  int buf = 6, pbuf = 3;
#pragma unroll 1
  for (int c = 0; c < 16; ++c) {
    const int nbuf = buf == 6 ? 0 : buf + 1;
    const char* sb = smem + buf * 10240;
    tile(kf[0], kf[1], vf[1], vf[0], sb, sb + 2560, true, c + 4, pbuf, 0);
    tile(kf[1], kf[0], vf[0], vf[1], sb + 2560, sb + 5120, true, c + 4, pbuf, 1);
    tile(kf[0], kf[1], vf[1], vf[0], sb + 5120, sb + 7680, true, c + 4, pbuf, 2);
    if (three) WAIT_VM(9); else WAIT_VM(6);
    BARRIER();
    tile(kf[1], kf[0], vf[0], vf[1], sb + 7680, smem + nbuf * 10240, false, 0, 0, 0);
    buf = nbuf;
    pbuf = pbuf == 6 ? 0 : pbuf + 1;
  }
  WAIT_VM0();
  o[1] = MFMA32(vf[1].v0, __builtin_bit_cast(bf16x8, pB[0]), o[1]);
  o[1] = MFMA32(vf[1].v1, __builtin_bit_cast(bf16x8, pB[1]), o[1]);
}

DI void k1_attn(char* smem, int id, const u16* __restrict__ wq, char* __restrict__ ws) {
  const int tid = TID, l = tid & 63;
  const int w = __builtin_amdgcn_readfirstlane(tid >> 6);
  const int r = l & 31, h = l >> 5;
  const int tb = (id & 7) * 4 + ((id >> 3) & 3), hg = id >> 5;
  const int head = hg * 4 + w, tt0 = tb * 2;
  bf16x8 q0[2];
  u32x4 q1[2];
  __builtin_amdgcn_s_setprio(3);
  {
    unsigned qo[3];
#pragma unroll
    for (int i = 0; i < 3; ++i) {
      const int idx = (w + 4 * i) * 64 + l, row = idx / 12, c = idx - row * 12, hd = c / 3, sub = c - hd * 3;
      qo[i] = row * 1152 + hd * 36 + (sub == 2 ? 20 : sub * 16);
    }
    const char* qsrc = (const char*)wq + hg * 144;
    const char* ysrc = ws + WS_YF + ((size_t)tt0 * 36 + w) * 1024;
    const unsigned lb = lds_addr(smem) + w * 1024, loff = l * 16;
    auto issue = [&](int st, int slot) {
      const unsigned d = lb + slot * 20480;
#pragma unroll
      for (int i = 0; i < 3; ++i) glds16s(qo[i], qsrc + (size_t)st * 64 * 1152, d + i * 4096);
      glds16s(loff, ysrc + st * 4096, d + 12288);
      glds16s(loff, ysrc + st * 4096 + 36 * 1024, d + 16384);
    };
#pragma unroll
    for (int i = 0; i < 3; ++i)
      if (i < 2 || w < 2) glds16s(loff, ws + WS_KV + w * 1024 + i * 4096, lb + 61440 + i * 4096);
    issue(0, 0); issue(1, 1);
    const int g = l >> 4, q = (l & 15) >> 2, p = l & 3;
    const int aq = (4 * h + q) * 192 + w * 48 + (g & 1) * 32 + p * 8;
    f32x16 qa[2];
#pragma unroll
    for (int i = 0; i < 16; ++i) { qa[0][i] = 0.f; qa[1][i] = 0.f; }
    int slot = 0;
#pragma unroll 1
    for (int st = 0; st < 9; ++st) {
      if (st < 8) WAIT_VM(5); else WAIT_VM(0);
      BARRIER();
      const char* sb = smem + slot * 20480;
      bf16x8 wf[4], yf[2][4];
#pragma unroll
      for (int ks = 0; ks < 4; ++ks) {
        wf[ks] = tr_frag(sb + aq + ks * 3072, sb + aq + ks * 3072 + 1536);
        yf[0][ks] = lds_frag(sb + 12288 + ks * 1024 + loff);
        yf[1][ks] = lds_frag(sb + 16384 + ks * 1024 + loff);
      }
      SCHED_FENCE();
      if (st < 7) issue(st + 2, slot == 0 ? 2 : slot - 1);
      SCHED_FENCE();
#pragma unroll
      for (int ks = 0; ks < 4; ++ks) { qa[0] = MFMA32(wf[ks], yf[0][ks], qa[0]); qa[1] = MFMA32(wf[ks], yf[1][ks], qa[1]); }
      slot = slot == 2 ? 0 : slot + 1;
    }
    const float* rinv = (const float*)(ws + WS_RINV);
#pragma unroll
    for (int qt = 0; qt < 2; ++qt) {
      const float a = rinv[(tt0 + qt) * 32 + r] * (1.4426950408889634f * 0.23570226039551584f);
      const u32x4 u = {pack2(qa[qt][0] * a, qa[qt][1] * a), pack2(qa[qt][2] * a, qa[qt][3] * a),
                       pack2(qa[qt][4] * a, qa[qt][5] * a), pack2(qa[qt][6] * a, qa[qt][7] * a)};
      q0[qt] = __builtin_bit_cast(bf16x8, u);
      q1[qt] = (u32x4){h == 1 ? pack2(qa[qt][10] * a, qa[qt][11] * a) : 0u, 0u, 0u, 0u};
    }
    syncthreads();
  }
  __builtin_amdgcn_s_setprio(1);
  f32x16 o[2];
  const char* kvimg = ws + WS_KV;
  attn_loop_fast(smem, kvimg, w, l, q0, q1, o);
  bool bad = false;
#pragma unroll
  for (int qt = 0; qt < 2; ++qt) {
    bad |= !(o[qt][10] <= 1.8446744e19f);
#pragma unroll
    for (int i = 0; i < 10; ++i) bad |= !(__builtin_fabsf(o[qt][i]) <= 3.0e38f);
  }
  volatile LDSP unsigned* flag = (volatile LDSP unsigned*)(smem + 73728 + 2048);
  if (tid == 0) *flag = 0u;
  syncthreads();
  if (bad) *flag = 1u;
  syncthreads();
  if (__builtin_amdgcn_readfirstlane(*flag) != 0u) {
    q1[0][1] = 0u; q1[1][1] = 0u;
    attn_loop_safe(smem, kvimg, w, l, q0, q1, o);
  }
  const auto rs = __builtin_amdgcn_make_buffer_rsrc(ws + WS_OF, 0, 64 * 36 * 1024, 0x00020000);
#pragma unroll
  for (int qt = 0; qt < 2; ++qt) {
    const float inv = 1.0f / o[qt][10];
    const unsigned dst = (tt0 + qt) * 36 * 1024;
    u32x4 u = {pack2(o[qt][0] * inv, o[qt][1] * inv), pack2(o[qt][2] * inv, o[qt][3] * inv),
               pack2(o[qt][4] * inv, o[qt][5] * inv), pack2(o[qt][6] * inv, o[qt][7] * inv)};
    __builtin_amdgcn_raw_buffer_store_b128(u, rs, dst + head * 1024 + l * 16, 0, 16);
    if (h == 0)
      __builtin_amdgcn_raw_buffer_store_b32(pack2(o[qt][8] * inv, o[qt][9] * inv), rs,
          dst + (32 + (head >> 3)) * 1024 + (((head >> 2) & 1) * 32 + r) * 16 + (head & 3) * 4, 0, 16);
  }
  WAIT_VM0();
  syncthreads();
  if (tid == 0) __hip_atomic_store((GAS unsigned*)(ws + WS_OFLG) + (tb * 8 + hg) * 16, 1u, __ATOMIC_RELAXED, __HIP_MEMORY_SCOPE_AGENT);
}

DI void k1_mlp(char* smem, int id, const u16* __restrict__ w1, const u16* __restrict__ w2,
               const u16* __restrict__ wo, const u16* __restrict__ x, const u16* __restrict__ b2,
               char* __restrict__ ws, u16* __restrict__ out) {
  const int tid = TID, l = tid & 63;
  const int w = __builtin_amdgcn_readfirstlane(tid >> 6);
  const int xcd = id & 7, j = id >> 3, rh = xcd >> 2, sq = xcd & 3;
  const int rb = rh * 8 + (j & 7), cb = sq * 4 + (j >> 3), nh = cb & 1;
  const int ttile = rb * 4 + w;
  const int h = l >> 5;
  const bool five = w < 2;
  unsigned woff1[5], woff2[5];
#pragma unroll
  for (int jj = 0; jj < 5; ++jj) {
    const int idx = (w + 4 * jj < 17 ? w + 4 * jj : 17) * 64 + l;
    const unsigned prow = idx / 36, pcol = (idx - prow * 36) * 16;
    woff1[jj] = prow * 9216 + pcol; woff2[jj] = prow * 1152 + pcol;
  }
  const char* w1src = (const char*)w1 + cb * 576;
  const char* w2src = (const char*)w2 + nh * 576;
  const char* ysrc = ws + WS_YF + (size_t)ttile * 36 * 1024;
  const char* hsrc = ws + WS_HF + (size_t)ttile * 288 * 1024;
  const char* osrc = ws + WS_OF + (size_t)ttile * 36 * 1024;
  const int pg = cb >> 1;
  const unsigned loff = l * 16;
  const unsigned lbase = lds_addr(smem) + w * 1024;
  auto wpiece = [&](const char* g, unsigned dst, int jj) {
    if (jj < 4 || five) glds16s(woff1[jj], g, dst + jj * 4096);
  };
  auto bload = [&](const char* p, u32x4 (&d)[2]) {
    asm volatile("global_load_dwordx4 %0, %2, %3 sc1\n\tglobal_load_dwordx4 %1, %2, %3 offset:1024 sc1"
                 : "=&v"(d[0]), "=&v"(d[1]) : "v"(loff), "s"(p) : "memory");
  };
  const char* wp = w1src + (size_t)3 * 32 * 9216;
  const char* bp = ysrc + 2 * 2048;
  unsigned wstride = 32 * 9216, bstride = 2048;
  const int g = l >> 4, q = (l & 15) >> 2, p = l & 3;
  const int aoff = (4 * h + q) * 576 + (g & 1) * 32 + p * 8;
  f32x16 acc[9];
  bf16x8 fa[5], fb[4];
  u32x4 br[3][2];
  auto rd = [&](const char* sb, int m) { return tr_frag(sb + m * 64, sb + m * 64 + 8 * 576); };
  auto touch_a = [&]() { asm volatile("" : "+v"(fa[0]), "+v"(fa[1]), "+v"(fa[2]), "+v"(fa[3]), "+v"(fa[4])); };
  auto touch_b = [&]() { asm volatile("" : "+v"(fb[0]), "+v"(fb[1]), "+v"(fb[2]), "+v"(fb[3])); };
  auto group_a = [&](const u32x4& b, const char* sb, auto job0, auto job1) {
    const bf16x8 bb = __builtin_bit_cast(bf16x8, b);
    touch_a();
    SCHED_FENCE();
    acc[0] = MFMA32(fa[0], bb, acc[0]); SCHED_FENCE();
    fb[0] = rd(sb, 5); fb[1] = rd(sb, 6); SCHED_FENCE();
    acc[1] = MFMA32(fa[1], bb, acc[1]); SCHED_FENCE();
    fb[2] = rd(sb, 7); fb[3] = rd(sb, 8); SCHED_FENCE();
    acc[2] = MFMA32(fa[2], bb, acc[2]); SCHED_FENCE();
    job0(); SCHED_FENCE();
    acc[3] = MFMA32(fa[3], bb, acc[3]); SCHED_FENCE();
    job1(); SCHED_FENCE();
    acc[4] = MFMA32(fa[4], bb, acc[4]); SCHED_FENCE();
  };
  auto group_b = [&](const u32x4& b, const char* sb, auto job0, auto job1) {
    const bf16x8 bb = __builtin_bit_cast(bf16x8, b);
    SCHED_FENCE();
    acc[5] = MFMA32(fb[0], bb, acc[5]); SCHED_FENCE();
    fa[0] = rd(sb, 0); fa[1] = rd(sb, 1); fa[2] = rd(sb, 2); SCHED_FENCE();
    acc[6] = MFMA32(fb[1], bb, acc[6]); SCHED_FENCE();
    fa[3] = rd(sb, 3); fa[4] = rd(sb, 4); SCHED_FENCE();
    acc[7] = MFMA32(fb[2], bb, acc[7]); SCHED_FENCE();
    job0(); SCHED_FENCE();
    acc[8] = MFMA32(fb[3], bb, acc[8]); SCHED_FENCE();
    job1(); SCHED_FENCE();
  };
  auto nojob = []() {};
  auto step = [&](int ph, int i, auto cc) {
    constexpr int C = decltype(cc)::value;
    const int n = ph * 18 + i;
    if (C == 2 && n == 20) {
      if (l == 0) __hip_atomic_store((GAS unsigned*)(ws + WS_FLG) + ((rb * 16 + cb) * 4 + w) * 16, 1u, __ATOMIC_RELAXED, __HIP_MEMORY_SCOPE_AGENT);
    }
    auto poll = [&](const char* fp) {
      GAS unsigned* f = (GAS unsigned*)fp;
      for (unsigned spins = 0; spins < (1u << 22); ++spins) {
        if (__builtin_amdgcn_readfirstlane(__hip_atomic_load(f, __ATOMIC_RELAXED, __HIP_MEMORY_SCOPE_AGENT)) != 0u) break;
        __builtin_amdgcn_s_sleep(2);
      }
    };
    if (C == 1 && n == 25) poll(ws + WS_FLG + ((rb * 16 + (cb ^ 1)) * 4 + w) * 64);
    if (C == 1 && n == 34) {
      poll(ws + WS_OFLG + ((ttile >> 1) * 8 + pg) * 64);
      poll(ws + WS_OFLG + ((ttile >> 1) * 8 + (pg ^ 1)) * 64);
    }
    const unsigned wd = lbase + ((n + 3) & 3) * 18432;
    const char* sb = smem + (n & 3) * 18432 + aoff;
    const char* sn = smem + ((n + 1) & 3) * 18432 + aoff;
    group_a(br[C][0], sb, [&]() { bload(bp, br[(C + 2) % 3]); }, [&]() { wpiece(wp, wd, 0); });
    touch_b();
    group_b(br[C][0], sb + 16 * 576, [&]() { wpiece(wp, wd, 1); }, [&]() { wpiece(wp, wd, 2); });
    group_a(br[C][1], sb + 16 * 576, [&]() { wpiece(wp, wd, 3); }, [&]() { wpiece(wp, wd, 4); });
    touch_b();
    if (five) WAIT_VM(12); else WAIT_VM(10);
    asm volatile("" : "+v"(br[(C + 1) % 3][0]), "+v"(br[(C + 1) % 3][1]));
    if (C == 1 && n == 37) {
      const u32x4 z = {0u, 0u, 0u, 0u};
      const u32x4 t = (pg >> 1) == 3 ? br[2][1] : br[2][0];
      br[2][0] = (pg & 1) ? z : t;
      br[2][1] = z;
    }
    BARRIER();
    group_b(br[C][1], sn, [&]() {
      wp += wstride;
      if (C == 2 && n == 14) {
        wp = w2src + (size_t)cb * 288 * 1152; wstride = 32 * 1152;
#pragma unroll
        for (int jj = 0; jj < 5; ++jj) woff1[jj] = woff2[jj];
      }
      if (C == 2 && n == 23) wp = w2src + (size_t)(cb ^ 1) * 288 * 1152;
      if (C == 2 && n == 32) {
        wp = (const char*)wo + nh * 576 + (size_t)pg * 72 * 1152; wstride = 36 * 1152;
#pragma unroll
        for (int jj = 0; jj < 5; ++jj) { const unsigned pr = woff2[jj] / 1152u; woff1[jj] = woff2[jj] + (pr >> 4) * 2 * 1152; }
      }
      if (C == 1 && n == 34) {
        wp = (const char*)wo + nh * 576 + (size_t)(pg >> 1) * 144 * 1152; wstride = 0;
#pragma unroll
        for (int jj = 0; jj < 5; ++jj) {
          const unsigned pr = woff2[jj] / 1152u, ii = pr & 15, j2 = 4 * (ii >> 3) + (ii & 3);
          woff1[jj] = woff2[jj] - pr * 1152u + (18 * (4 * ((ii >> 2) & 1) + (j2 >> 1)) + 16 + (j2 & 1)) * 1152u;
        }
      }
      if (C == 0 && n == 15) bstride = 0;
      bp += bstride;
      if (C == 0 && n == 24) bp = hsrc + (size_t)(cb ^ 1) * 18 * 1024;
      if (C == 0 && n == 33) bp = osrc + pg * 4 * 1024;
      if (C == 2 && n == 35) { bp = osrc + (32 + (pg >> 1)) * 1024 - ((pg >> 1) == 3 ? 1024 : 0); bstride = 0; }
    }, nojob);
  };
  __builtin_amdgcn_s_setprio(2);
  if (w == 0) glds16s(loff, ws + WS_B1F + cb * 1152, lds_addr(smem) + 73728);
  if (w == 1 && l < 8) glds16s(loff, ws + WS_B1F + cb * 1152 + 1024, lds_addr(smem) + 73728 + 1024);
#pragma unroll
  for (int st = 0; st < 3; ++st) {
#pragma unroll
    for (int jj = 0; jj < 5; ++jj) wpiece(w1src + (size_t)st * 32 * 9216, lbase + st * 18432, jj);
    if (st < 2) bload(ysrc + st * 2048, br[st]);
  }
  if (five) WAIT_VM(12); else WAIT_VM(10);
  BARRIER();
#pragma unroll
  for (int m = 0; m < 9; ++m)
#pragma unroll
    for (int gq = 0; gq < 4; ++gq) {
      const f32x4 bv = *(const f32x4*)(smem + 73728 + (m * 32 + 8 * gq + 4 * h) * 4);
#pragma unroll
      for (int e = 0; e < 4; ++e) acc[m][4 * gq + e] = bv[e];
    }
  const auto rp = __builtin_amdgcn_make_buffer_rsrc(ws + WS_PART, 0, 64 * 18 * 16 * 1024, 0x00020000);
  int nph = 2;
  asm volatile("" : "+s"(nph));
#pragma unroll 1
  for (int ph = 0; ph < nph; ++ph) {
    asm volatile("" : "+v"(br[0][0]), "+v"(br[0][1]));
    {
      const char* s0 = smem + ph * 2 * 18432 + aoff;
#pragma unroll
      for (int m = 0; m < 5; ++m) fa[m] = rd(s0, m);
    }
#pragma unroll 1
    for (int i = 0; i < 18 + 3 * ph; i += 3) { step(ph, i, IC(0)); step(ph, i + 1, IC(1)); step(ph, i + 2, IC(2)); }
    if (ph == 0) {
      const float k0 = -2.0f * 0.7978845608028654f * 1.4426950408889634f, k1 = k0 * 0.044715f;
      const auto rs = __builtin_amdgcn_make_buffer_rsrc(ws + WS_HF, 0, 64 * 288 * 1024, 0x00020000);
      const unsigned hbase = (ttile * 288 + cb * 18) * 1024 + l * 16;
      u32x4 hk[2][2];
#pragma unroll
      for (int m = 0; m < 9; ++m) {
#pragma unroll
        for (int s = 0; s < 2; ++s) {
          u32x4 u;
#pragma unroll
          for (int e = 0; e < 4; ++e)
            u[e] = pack2(gelu_tanh(acc[m][8 * s + 2 * e], k0, k1), gelu_tanh(acc[m][8 * s + 2 * e + 1], k0, k1));
          __builtin_amdgcn_raw_buffer_store_b128(u, rs, hbase + (m * 2 + s) * 1024, 0, 16);
          if (m < 2) hk[m][s] = u;
        }
      }
      WAIT_VM(18);
      asm volatile("" :: "v"(br[0][0]), "v"(br[0][1]), "v"(br[1][0]), "v"(br[1][1]));
      __builtin_amdgcn_s_setprio(0);
      br[0][0] = hk[0][0]; br[0][1] = hk[0][1]; br[1][0] = hk[1][0]; br[1][1] = hk[1][1];
      bp = hsrc + (size_t)cb * 18 * 1024 + 4096; bstride = 2048;
#pragma unroll
      for (int m = 0; m < 9; ++m)
#pragma unroll
        for (int i = 0; i < 16; ++i) acc[m][i] = 0.f;
    }
  }
#pragma unroll
  for (int m = 0; m < 9; ++m)
#pragma unroll
    for (int s = 0; s < 2; ++s) {
      const u32x4 u = {pack2(acc[m][8 * s], acc[m][8 * s + 1]), pack2(acc[m][8 * s + 2], acc[m][8 * s + 3]),
                       pack2(acc[m][8 * s + 4], acc[m][8 * s + 5]), pack2(acc[m][8 * s + 6], acc[m][8 * s + 7])};
      __builtin_amdgcn_raw_buffer_store_b128(u, rp, (((ttile * 18 + nh * 9 + m) * 2 + s) * 8 + pg) * 1024 + l * 16, 0, 16);
    }
  const int ft = rb * 4 + (pg >> 1), fs = pg & 1;
  const int nm = w == 0 ? 3 : 2, m0 = w == 0 ? 0 : 2 * w + 1;
  const size_t obase = (size_t)(ft * 32 + (l & 31)) * DM + nh * 288 + m0 * 32 + 16 * fs + 4 * h;
  u32x2 xv[3][2], bv[3][2];
#pragma unroll
  for (int m = 0; m < 3; ++m)
#pragma unroll
    for (int g2 = 0; g2 < 2; ++g2) {
      const int mm = m < nm ? m : 0;
      xv[m][g2] = *(const u32x2*)(x + obase + mm * 32 + 8 * g2);
      bv[m][g2] = *(const u32x2*)(b2 + nh * 288 + (m0 + mm) * 32 + 16 * fs + 4 * h + 8 * g2);
    }
  signal(ws, CNT_P + rb * 2 + nh);
  asm volatile("" :: "v"(br[0][0]), "v"(br[0][1]), "v"(br[1][0]), "v"(br[1][1]));
  await(ws, CNT_P + rb * 2 + nh, 8u);
  u32x4 pv[3][8];
#pragma unroll
  for (int m = 0; m < 3; ++m)
#pragma unroll
    for (int src = 0; src < 8; ++src)
      pv[m][src] = __builtin_amdgcn_raw_buffer_load_b128(
          rp, (((ft * 18 + nh * 9 + m0 + (m < nm ? m : 0)) * 2 + fs) * 8 + src) * 1024 + l * 16, 0, 16);
#pragma unroll
  for (int m = 0; m < 3; ++m) {
    float v[8];
#pragma unroll
    for (int g2 = 0; g2 < 2; ++g2) {
      v[4 * g2 + 0] = bflo(xv[m][g2][0]) + bflo(bv[m][g2][0]);
      v[4 * g2 + 1] = bfhi(xv[m][g2][0]) + bfhi(bv[m][g2][0]);
      v[4 * g2 + 2] = bflo(xv[m][g2][1]) + bflo(bv[m][g2][1]);
      v[4 * g2 + 3] = bfhi(xv[m][g2][1]) + bfhi(bv[m][g2][1]);
    }
#pragma unroll
    for (int src = 0; src < 8; ++src)
#pragma unroll
      for (int e = 0; e < 4; ++e) { const unsigned u = pv[m][src][e]; v[2 * e] += bflo(u); v[2 * e + 1] += bfhi(u); }
    if (m < nm) {
#pragma unroll
      for (int g2 = 0; g2 < 2; ++g2)
        *(u32x2*)(out + obase + m * 32 + 8 * g2) = (u32x2){pack2(v[4 * g2], v[4 * g2 + 1]), pack2(v[4 * g2 + 2], v[4 * g2 + 3])};
    }
  }
}

__global__ __launch_bounds__(256, 2) void k1_kernel(const u16* __restrict__ wq,
                                                    const u16* __restrict__ w1, const u16* __restrict__ w2,
                                                    const u16* __restrict__ wo, const u16* __restrict__ x,
                                                    const u16* __restrict__ b2, char* __restrict__ ws,
                                                    u16* __restrict__ out) {
  extern __shared__ __attribute__((aligned(16))) char smem[];
  const int b = BID;
  if (b < 256) {
    const char* pc = (const char*)__builtin_amdgcn_s_getpc();
    unsigned d0, d1;
    const unsigned off = TID * 64;
    asm volatile("global_load_dword %0, %2, %3\n\tglobal_load_dword %1, %4, %3"
                 : "=&v"(d0), "=&v"(d1) : "v"(off), "s"(pc), "v"(off < K1_CODE - 16384 ? off + 16384 : off) : "memory");
    k1_attn(smem, b, wq, ws);
    asm volatile("" :: "v"(d0), "v"(d1));
  }
  else k1_mlp(smem, b - 256, w1, w2, wo, x, b2, ws, out);
}

#if defined(__HIP_DEVICE_COMPILE__)
#pragma clang attribute pop
#endif

extern "C" void kernel_launch(void* const* d_in, const int* in_sizes, int n_in, void* d_out, int out_size,
                              void* d_ws, size_t ws_size, hipStream_t stream) {
  const u16* x = (const u16*)d_in[0];
  const u16* wq = (const u16*)d_in[1];
  const u16* wk = (const u16*)d_in[2];
  const u16* wv = (const u16*)d_in[3];
  const u16* wo = (const u16*)d_in[4];
  const u16* w1 = (const u16*)d_in[6];
  const u16* b1 = (const u16*)d_in[7];
  const u16* w2 = (const u16*)d_in[8];
  const u16* b2 = (const u16*)d_in[9];
  char* ws = (char*)d_ws;
  (void)hipFuncSetAttribute((const void*)k1_kernel, hipFuncAttributeMaxDynamicSharedMemorySize, K1_LDS);
  (void)hipFuncSetAttribute((const void*)k0_kernel, hipFuncAttributeMaxDynamicSharedMemorySize, K0_LDS);
  k0_kernel<<<192, 512, K0_LDS, stream>>>(x, wk, wv, b1, ws);
  k1_kernel<<<512, 256, K1_LDS, stream>>>(wq, w1, w2, wo, x, b2, ws, (u16*)d_out);
}
```

```cpp
#include <hip/hip_runtime.h>
#include <hip/hip_bf16.h>
#include <stdint.h>
#include <type_traits>

#if defined(__HIP_DEVICE_COMPILE__)
#pragma clang attribute push(__attribute__((target("no-packed-fp32-ops"))), apply_to = function)
#endif

typedef short bf16x8 __attribute__((ext_vector_type(8)));
typedef short bf16x4 __attribute__((ext_vector_type(4)));
typedef __bf16 bfx2 __attribute__((ext_vector_type(2)));
typedef float f32x2 __attribute__((ext_vector_type(2)));
typedef float f32x4 __attribute__((ext_vector_type(4)));
typedef float f32x16 __attribute__((ext_vector_type(16)));
typedef unsigned u32x2 __attribute__((ext_vector_type(2)));
typedef unsigned u32x4 __attribute__((ext_vector_type(4)));
typedef unsigned u32x4_a8 __attribute__((ext_vector_type(4), aligned(8)));
typedef unsigned short u16;
#define LDSP __attribute__((address_space(3)))
#define GAS __attribute__((address_space(1)))
#define DI __device__ __forceinline__
#define MFMA32(a, b, c) __builtin_amdgcn_mfma_f32_32x32x16_bf16((a), (b), (c), 0, 0, 0)
#define IC(n) std::integral_constant<int, n>{}

constexpr int SEQ = 2048, DM = 576, FF = 4608;
constexpr float LN_EPS = 1e-5f;
constexpr size_t WS_EPOCH = 0;
constexpr size_t WS_RFLG = 4096;
constexpr size_t WS_YFLG = 8192;
constexpr size_t WS_KFLG = 16384;
constexpr size_t WS_PFLG = 24576;
constexpr size_t WS_OFLG = 98304;
constexpr size_t WS_FLG = 32768;
constexpr size_t WS_HF = 131072;
constexpr size_t WS_KV = WS_HF + (size_t)64 * 288 * 1024;
constexpr size_t WS_OF = WS_KV + (size_t)64 * 2560;
constexpr size_t WS_YF = WS_OF + (size_t)64 * 36 * 1024;
constexpr size_t WS_PART = WS_YF + (size_t)64 * 36 * 1024;

DI unsigned pack2(float a, float b) {
  f32x2 p = {a, b};
  return __builtin_bit_cast(unsigned, __builtin_convertvector(p, bfx2));
}
DI float bflo(unsigned u) { return __builtin_bit_cast(float, u << 16); }
DI float bfhi(unsigned u) { return __builtin_bit_cast(float, u & 0xffff0000u); }
DI void glds16s(unsigned voff, const void* sbase, unsigned lds) {
  unsigned keep;
  asm volatile("s_mov_b32 %0, m0\n\ts_mov_b32 m0, %3\n\ts_nop 0\n\tglobal_load_lds_dwordx4 %1, %2\n\ts_mov_b32 m0, %0"
               : "=&s"(keep) : "v"(voff), "s"(sbase), "s"(lds) : "memory");
}
DI void glds16s_sc1(unsigned voff, const void* sbase, unsigned lds) {
  unsigned keep;
  asm volatile("s_mov_b32 %0, m0\n\ts_mov_b32 m0, %3\n\ts_nop 0\n\tglobal_load_lds_dwordx4 %1, %2 sc1\n\ts_mov_b32 m0, %0"
               : "=&s"(keep) : "v"(voff), "s"(sbase), "s"(lds) : "memory");
}
DI float dot2(unsigned a, unsigned b, float c) {
  return __builtin_amdgcn_fdot2_f32_bf16(__builtin_bit_cast(bfx2, a), __builtin_bit_cast(bfx2, b), c, false);
}
DI unsigned lds_addr(const char* p) { return (unsigned)(size_t)(LDSP const char*)p; }
DI bf16x8 tr_frag(const char* lds_a, const char* lds_b) {
  bf16x4 a = __builtin_amdgcn_ds_read_tr16_b64_v4i16((LDSP bf16x4*)lds_a);
  bf16x4 b = __builtin_amdgcn_ds_read_tr16_b64_v4i16((LDSP bf16x4*)lds_b);
  return __builtin_shufflevector(a, b, 0, 1, 2, 3, 4, 5, 6, 7);
}
DI bf16x8 lds_frag(const char* p) { return *(const bf16x8*)p; }
DI float xhalf(float v) {
  const int lane = __builtin_amdgcn_mbcnt_hi(~0u, __builtin_amdgcn_mbcnt_lo(~0u, 0u));
  return __builtin_bit_cast(float, __builtin_amdgcn_ds_bpermute((lane ^ 32) << 2, __builtin_bit_cast(int, v)));
}
DI float xhalf_sum(float v) { return v + xhalf(v); }
DI float xhalf_max(float v) { return __builtin_fmaxf(v, xhalf(v)); }
DI void syncthreads() {
  __builtin_amdgcn_fence(__ATOMIC_RELEASE, "workgroup");
  __builtin_amdgcn_s_barrier();
  __builtin_amdgcn_fence(__ATOMIC_ACQUIRE, "workgroup");
}
#define TID ((int)__builtin_amdgcn_workitem_id_x())
#define BID ((int)__builtin_amdgcn_workgroup_id_x())
#define WAIT_VM0() asm volatile("s_waitcnt vmcnt(0)" ::: "memory")
#define WAIT_VM(n) asm volatile("s_waitcnt vmcnt(" #n ")" ::: "memory")
#define BARRIER() __builtin_amdgcn_s_barrier()
#define SCHED_FENCE() __builtin_amdgcn_sched_barrier(0)
#define ONES2 0x3f803f80u

typedef unsigned long long u64;
DI void set_flag(char* p, u64 tag) { __hip_atomic_store((GAS u64*)p, tag, __ATOMIC_RELAXED, __HIP_MEMORY_SCOPE_AGENT); }
DI void wait_flags(const char* base, int n, u64 tag, int nap) {
  const int l = TID & 63;
  GAS u64* f = (GAS u64*)base + (l < n ? l : 0);
#pragma unroll 1
  for (unsigned spins = 0; spins < (1u << 21); ++spins) {
    const u64 v = __hip_atomic_load(f, __ATOMIC_RELAXED, __HIP_MEMORY_SCOPE_AGENT);
    if (__builtin_amdgcn_ballot_w64(v != tag) == 0) break;
    if (nap > 6) __builtin_amdgcn_s_sleep(24); else if (nap > 4) __builtin_amdgcn_s_sleep(10); else __builtin_amdgcn_s_sleep(3);
  }
}
DI float gelu_tanh(float x, float k0, float k1) {
  const float z = x * __builtin_fmaf(x * x, k1, k0);
  return x * __builtin_amdgcn_rcpf(1.0f + __builtin_amdgcn_exp2f(z));
}

template <int CTRL> DI float dpp_f(float v) {
  return __builtin_bit_cast(float, __builtin_amdgcn_update_dpp(0, __builtin_bit_cast(int, v), CTRL, 0xf, 0xf, true));
}
DI float sum8(float v) {
  v += dpp_f<0xB1>(v);
  v += dpp_f<0x4E>(v);
  v += dpp_f<0x141>(v);
  return v;
}
DI void ln8(const u16* __restrict__ x, int tok0, char* __restrict__ ws) {
  const int l = TID & 63, c8 = l & 7, tok = tok0 + (l >> 3);
  u32x4 v[9];
#pragma unroll
  for (int i = 0; i < 9; ++i) v[i] = *(const u32x4*)((const char*)x + (size_t)tok * 1152 + (c8 + 8 * i) * 16);
  float f[72];
  float sx = 0.f;
#pragma unroll
  for (int i = 0; i < 9; ++i)
#pragma unroll
    for (int e = 0; e < 4; ++e) {
      f[8 * i + 2 * e] = bflo(v[i][e]); f[8 * i + 2 * e + 1] = bfhi(v[i][e]);
      sx += f[8 * i + 2 * e] + f[8 * i + 2 * e + 1];
    }
  const float m = sum8(sx) * (1.0f / DM);
  float sq = 0.f;
#pragma unroll
  for (int i = 0; i < 72; ++i) { f[i] -= m; sq = __builtin_fmaf(f[i], f[i], sq); }
  const float var = sum8(sq) * (1.0f / DM);
  const float r1 = 1.0f / __builtin_sqrtf(var + LN_EPS);
  const float s2 = __builtin_sqrtf(var * r1 * r1 + LN_EPS);
  const float a2 = r1 / s2;
  const bool odd = c8 & 1;
  const auto ry = __builtin_amdgcn_make_buffer_rsrc(ws + WS_YF, 0, 64 * 36 * 1024, 0x00020000);
  const unsigned dst = ((tok >> 5) * 36 + (c8 >> 1)) * 1024 + ((c8 & 1) * 32 + (tok & 31)) * 16;
#pragma unroll
  for (int i = 0; i < 9; ++i) {
    unsigned u[4];
#pragma unroll
    for (int e = 0; e < 4; ++e) u[e] = pack2(f[8 * i + 2 * e] * a2, f[8 * i + 2 * e + 1] * a2);
    const unsigned s0 = odd ? u[0] : u[2], s1 = odd ? u[1] : u[3];
    const unsigned r0 = __builtin_amdgcn_update_dpp(0, (int)s0, 0xB1, 0xf, 0xf, true);
    const unsigned r1u = __builtin_amdgcn_update_dpp(0, (int)s1, 0xB1, 0xf, 0xf, true);
    const u32x4 o = odd ? (u32x4){r0, r1u, u[2], u[3]} : (u32x4){u[0], u[1], r0, r1u};
    __builtin_amdgcn_raw_buffer_store_b128(o, ry, dst + i * 4096, 0, 16);
  }
}

constexpr int K1_LDS = 73728 + 2048 + 64;
constexpr unsigned K1_CODE = 28672;

struct KFrag { bf16x8 k0; u32x4 k1; };
struct VFrag { bf16x8 v0, v1; };

DI void attn_loop_safe(char* smem, const char* __restrict__ kvimg, int w, int l, const bf16x8 (&q0)[2],
                       const u32x4 (&q1)[2], f32x16 (&o)[2]) {
  const int r = l & 31, h = l >> 5;
  const int dv = (r == 22) ? 18 : (r >= 20 ? 19 : r);
  const int koff = l * 16, k1off = 1024 + r * 8, voff = 1280 + (h * 20 + dv) * 16;
  auto stage = [&](int c) {
#pragma unroll
    for (int i = 0; i < 5; ++i)
      glds16s_sc1(l * 16, kvimg + (size_t)c * 20480 + (5 * w + i) * 1024, lds_addr(smem) + (c & 1) * 20480 + (5 * w + i) * 1024);
  };
  float mrun[2] = {-3.0e38f, -3.0e38f};
#pragma unroll
  for (int qt = 0; qt < 2; ++qt)
#pragma unroll
    for (int i = 0; i < 16; ++i) o[qt][i] = 0.f;
  const f32x16 zero = {};
  stage(0);
#pragma unroll 1
  for (int c = 0; c < 8; ++c) {
    WAIT_VM0();
    BARRIER();
    if (c + 1 < 8) stage(c + 1);
    const char* sb = smem + (c & 1) * 20480;
#pragma unroll 1
    for (int t = 0; t < 8; ++t) {
      const char* tb = sb + t * 2560;
      const bf16x8 kf0 = lds_frag(tb + koff);
      const u32x2 k1 = *(const u32x2*)(tb + k1off);
      const u32x4 k1v = {k1[0], k1[1], 0u, 0u};
      const bf16x8 vf0 = lds_frag(tb + voff), vf1 = lds_frag(tb + voff + 640);
#pragma unroll
      for (int qt = 0; qt < 2; ++qt) {
        f32x16 s = MFMA32(kf0, q0[qt], zero);
        s = MFMA32(__builtin_bit_cast(bf16x8, k1v), __builtin_bit_cast(bf16x8, q1[qt]), s);
        float m = s[0];
#pragma unroll
        for (int i = 1; i < 16; ++i) m = __builtin_fmaxf(m, s[i]);
        m = xhalf_max(m);
        const float mn = __builtin_fmaxf(mrun[qt], m);
        const float alpha = __builtin_amdgcn_exp2f(mrun[qt] - mn);
        mrun[qt] = mn;
#pragma unroll
        for (int i = 0; i < 16; ++i) { o[qt][i] *= alpha; s[i] -= mn; }
        unsigned p[8];
#pragma unroll
        for (int i = 0; i < 8; ++i)
          p[i] = pack2(__builtin_amdgcn_exp2f(s[2 * i]), __builtin_amdgcn_exp2f(s[2 * i + 1]));
        const u32x4 pa = {p[0], p[1], p[2], p[3]}, pb = {p[4], p[5], p[6], p[7]};
        o[qt] = MFMA32(vf0, __builtin_bit_cast(bf16x8, pa), o[qt]);
        o[qt] = MFMA32(vf1, __builtin_bit_cast(bf16x8, pb), o[qt]);
      }
    }
  }
}

DI void attn_loop_fast(char* smem, const char* __restrict__ kvimg, int w, int l, const bf16x8 (&q0)[2],
                       u32x4 (&q1)[2], f32x16 (&o)[2]) {
  const int r = l & 31, h = l >> 5;
  const int dv = (r == 22) ? 18 : (r >= 20 ? 19 : r);
  const int koff = l * 16, k1off = 1024 + r * 8, voff = 1280 + (h * 20 + dv) * 16;
  const char* gsrc = kvimg + w * 1024;
  const unsigned lbase = lds_addr(smem) + w * 1024, loff = l * 16;
  const bool three = w < 2;
  auto piece = [&](int c, int buf, int i) {
    if (i < 2 || three) glds16s_sc1(loff, gsrc + (size_t)(c < 15 ? c : 15) * 10240 + i * 4096, lbase + buf * 10240 + i * 4096);
  };
  const f32x16 zero = {};
  auto rdk = [&](KFrag& k, const char* tb) {
    k.k0 = lds_frag(tb + koff);
    k.k1 = *(const u32x4_a8*)(tb + k1off);
  };
  auto rdv = [&](VFrag& v, const char* tb) { v.v0 = lds_frag(tb + voff); v.v1 = lds_frag(tb + voff + 640); };
  auto qk = [&](const KFrag& k, int qt) {
    f32x16 s = MFMA32(k.k0, q0[qt], zero);
    return MFMA32(__builtin_bit_cast(bf16x8, k.k1), __builtin_bit_cast(bf16x8, q1[qt]), s);
  };
#pragma unroll
  for (int qt = 0; qt < 2; ++qt)
#pragma unroll
    for (int i = 0; i < 16; ++i) o[qt][i] = 0.f;
#pragma unroll 1
  for (int c = 0; c < 4; ++c) { const int bf = c == 0 ? 6 : c - 1; piece(c, bf, 0); piece(c, bf, 1); piece(c, bf, 2); }
  if (three) WAIT_VM(9); else WAIT_VM(6);
  BARRIER();
  KFrag kf[2];
  VFrag vf[2];
  rdk(kf[0], smem + 6 * 10240);
#pragma unroll
  for (int qt = 0; qt < 2; ++qt) {
    const f32x16 s = qk(kf[0], qt);
    float m = s[0];
#pragma unroll
    for (int i = 1; i < 16; ++i) m = __builtin_fmaxf(m, s[i]);
    m = xhalf_max(m);
    if (h == 1) q1[qt][1] = pack2(-m, 0.f);
  }
  f32x16 sA = qk(kf[0], 0), sB;
  u32x4 pA[2], pB[2];
  pB[0] = (u32x4){0u, 0u, 0u, 0u}; pB[1] = pB[0];
  vf[1].v0 = __builtin_bit_cast(bf16x8, pB[0]); vf[1].v1 = vf[1].v0;
  auto expack = [&](const f32x16& s, u32x4 (&p)[2]) {
#pragma unroll
    for (int i = 0; i < 8; ++i)
      p[i >> 2][i & 3] = pack2(__builtin_amdgcn_exp2f(s[2 * i]), __builtin_amdgcn_exp2f(s[2 * i + 1]));
  };
  auto tile = [&](KFrag& kc, KFrag& kn, VFrag& vp, VFrag& vc, const char* tcur, const char* tnext, bool dma, int pc,
                  int pbuf, int pi) {
    {
      rdk(kn, tnext);
      rdv(vc, tcur);
      f32x16 s = MFMA32(kc.k0, q0[1], zero);
      o[1] = MFMA32(vp.v0, __builtin_bit_cast(bf16x8, pB[0]), o[1]);
      sB = MFMA32(__builtin_bit_cast(bf16x8, kc.k1), __builtin_bit_cast(bf16x8, q1[1]), s);
      o[1] = MFMA32(vp.v1, __builtin_bit_cast(bf16x8, pB[1]), o[1]);
      expack(sA, pA);
#pragma unroll
      for (int i = 0; i < 4; ++i) {
        __builtin_amdgcn_sched_group_barrier(0x008, 1, 0);
        if (i < 2) __builtin_amdgcn_sched_group_barrier(0x100, 2, 0);
        __builtin_amdgcn_sched_group_barrier(0x400, 4, 0);
        __builtin_amdgcn_sched_group_barrier(0x002, 2, 0);
      }
      asm volatile("" : "+v"(pA[0]), "+v"(pA[1]));
    }
    SCHED_FENCE();
    {
      f32x16 s = MFMA32(kn.k0, q0[0], zero);
      o[0] = MFMA32(vc.v0, __builtin_bit_cast(bf16x8, pA[0]), o[0]);
      sA = MFMA32(__builtin_bit_cast(bf16x8, kn.k1), __builtin_bit_cast(bf16x8, q1[0]), s);
      o[0] = MFMA32(vc.v1, __builtin_bit_cast(bf16x8, pA[1]), o[0]);
      expack(sB, pB);
#pragma unroll
      for (int i = 0; i < 4; ++i) {
        __builtin_amdgcn_sched_group_barrier(0x008, 1, 0);
        __builtin_amdgcn_sched_group_barrier(0x400, 4, 0);
        __builtin_amdgcn_sched_group_barrier(0x002, 2, 0);
      }
      asm volatile("" : "+v"(pB[0]), "+v"(pB[1]));
    }
    SCHED_FENCE();
    if (dma) piece(pc, pbuf, pi);
    SCHED_FENCE();
  };
  int buf = 6, pbuf = 3;
#pragma unroll 1
  for (int c = 0; c < 16; ++c) {
    const int nbuf = buf == 6 ? 0 : buf + 1;
    const char* sb = smem + buf * 10240;
    tile(kf[0], kf[1], vf[1], vf[0], sb, sb + 2560, true, c + 4, pbuf, 0);
    tile(kf[1], kf[0], vf[0], vf[1], sb + 2560, sb + 5120, true, c + 4, pbuf, 1);
    tile(kf[0], kf[1], vf[1], vf[0], sb + 5120, sb + 7680, true, c + 4, pbuf, 2);
    if (three) WAIT_VM(9); else WAIT_VM(6);
    BARRIER();
    tile(kf[1], kf[0], vf[0], vf[1], sb + 7680, smem + nbuf * 10240, false, 0, 0, 0);
    buf = nbuf;
    pbuf = pbuf == 6 ? 0 : pbuf + 1;
  }
  WAIT_VM0();
  o[1] = MFMA32(vf[1].v0, __builtin_bit_cast(bf16x8, pB[0]), o[1]);
  o[1] = MFMA32(vf[1].v1, __builtin_bit_cast(bf16x8, pB[1]), o[1]);
}

DI void attn_wg(char* smem, int id, const u16* __restrict__ x, const u16* __restrict__ wq, char* __restrict__ ws,
                u64 tag) {
  const int tid = TID, l = tid & 63;
  const int w = __builtin_amdgcn_readfirstlane(tid >> 6);
  const int r = l & 31, h = l >> 5;
  const int tb = (id & 7) * 4 + ((id >> 3) & 3), hg = id >> 5;
  const int head = hg * 4 + w, tt0 = tb * 2;
  bf16x8 q0[2];
  u32x4 q1[2];
  volatile LDSP unsigned* flag = (volatile LDSP unsigned*)(smem + 73728 + 2048);
  if (tid == 0) *flag = 0u;
  __builtin_amdgcn_s_setprio(3);
  {
    unsigned qo[3], xo[2];
#pragma unroll
    for (int i = 0; i < 3; ++i) {
      const int idx = (w + 4 * i) * 64 + l, row = idx / 12, c = idx - row * 12, hd = c / 3, sub = c - hd * 3;
      qo[i] = row * 1152 + hd * 36 + (sub == 2 ? 20 : sub * 16);
    }
#pragma unroll
    for (int i = 0; i < 2; ++i) {
      const int row = (w + 4 * i) * 8 + (l >> 3);
      xo[i] = row * 1152 + (((l & 7) ^ ((row >> 1) & 7)) << 4);
    }
    const char* qsrc = (const char*)wq + hg * 144;
    const char* xsrc = (const char*)x + (size_t)tb * 64 * 1152;
    const unsigned lb = lds_addr(smem) + w * 1024;
    auto issue = [&](int st, int slot) {
      const unsigned d = lb + slot * 24576;
#pragma unroll
      for (int i = 0; i < 3; ++i) glds16s(qo[i], qsrc + (size_t)st * 64 * 1152, d + i * 4096);
      glds16s(xo[0], xsrc + st * 128, d + 12288);
      glds16s(xo[1], xsrc + st * 128, d + 16384);
    };
    issue(0, 0); issue(1, 1);
    const int g = l >> 4, q = (l & 15) >> 2, p = l & 3;
    const int aq = (8 * h + q) * 192 + w * 48 + (g & 1) * 32 + p * 8;
    int bx[4];
#pragma unroll
    for (int ks = 0; ks < 4; ++ks) bx[ks] = 12288 + r * 128 + (((2 * ks + h) ^ ((r >> 1) & 7)) << 4);
    const int bs = 12288 + r * 128 + (((2 * w + h) ^ ((r >> 1) & 7)) << 4);
    f32x16 qa[2];
#pragma unroll
    for (int i = 0; i < 16; ++i) { qa[0][i] = 0.f; qa[1][i] = 0.f; }
    float sx[2] = {0.f, 0.f}, sxx[2] = {0.f, 0.f}, cq = 0.f;
    int slot = 0;
#pragma unroll 1
    for (int st = 0; st < 9; ++st) {
      if (st < 8) WAIT_VM(5); else WAIT_VM(0);
      BARRIER();
      const char* sb = smem + slot * 24576;
      bf16x8 wf[4], xf[2][4], sf[2];
#pragma unroll
      for (int ks = 0; ks < 4; ++ks) {
        wf[ks] = tr_frag(sb + aq + ks * 3072, sb + aq + ks * 3072 + 768);
        xf[0][ks] = lds_frag(sb + bx[ks]);
        xf[1][ks] = lds_frag(sb + bx[ks] + 4096);
      }
      sf[0] = lds_frag(sb + bs); sf[1] = lds_frag(sb + bs + 4096);
      SCHED_FENCE();
      if (st < 7) issue(st + 2, slot == 0 ? 2 : slot - 1);
      SCHED_FENCE();
#pragma unroll
      for (int ks = 0; ks < 4; ++ks) { qa[0] = MFMA32(wf[ks], xf[0][ks], qa[0]); qa[1] = MFMA32(wf[ks], xf[1][ks], qa[1]); }
#pragma unroll
      for (int t = 0; t < 2; ++t) {
        const u32x4 u = __builtin_bit_cast(u32x4, sf[t]);
#pragma unroll
        for (int e = 0; e < 4; ++e) { sx[t] = dot2(u[e], ONES2, sx[t]); sxx[t] = dot2(u[e], u[e], sxx[t]); }
      }
#pragma unroll
      for (int ks = 0; ks < 4; ++ks) {
        const u32x4 u = __builtin_bit_cast(u32x4, wf[ks]);
#pragma unroll
        for (int e = 0; e < 4; ++e) cq = dot2(u[e], ONES2, cq);
      }
      asm volatile("" : "+v"(sx[0]), "+v"(sx[1]), "+v"(sxx[0]), "+v"(sxx[1]), "+v"(cq));
      slot = slot == 2 ? 0 : slot + 1;
    }
    syncthreads();
    {
      char* red = smem + l * 16;
      *(f32x4*)(red + 12288 + w * 1024) = (f32x4){sx[0], sxx[0], sx[1], sxx[1]};
      cq = xhalf_sum(cq);
      if (h == 0) *(float*)(smem + 17408 + w * 128 + r * 4) = cq;
    }
    syncthreads();
    float a1[2], c1[2];
    {
      f32x4 t = {0.f, 0.f, 0.f, 0.f};
#pragma unroll
      for (int i = 0; i < 8; ++i) {
        const f32x4 v = *(const f32x4*)(smem + 12288 + (i >> 1) * 1024 + ((i & 1) * 32 + r) * 16);
        t[0] += v[0]; t[1] += v[1]; t[2] += v[2]; t[3] += v[3];
      }
#pragma unroll
      for (int i = 0; i < 2; ++i) {
        const float mean = t[2 * i] * (1.0f / DM);
        const float var = __builtin_fmaxf(t[2 * i + 1] * (1.0f / DM) - mean * mean, 0.0f);
        a1[i] = 1.0f / __builtin_sqrtf(var + LN_EPS); c1[i] = -mean * a1[i];
      }
    }
    {
      const float qs = 1.4426950408889634f * 0.23570226039551584f;
      f32x4 c4[3];
#pragma unroll
      for (int gg = 0; gg < 3; ++gg) c4[gg] = *(const f32x4*)(smem + 17408 + w * 128 + (8 * gg + 4 * h) * 4);
#pragma unroll
      for (int qt = 0; qt < 2; ++qt) {
        const float a = a1[qt] * qs, c = c1[qt] * qs;
        float v[12];
#pragma unroll
        for (int i = 0; i < 12; ++i) v[i] = __builtin_fmaf(a, qa[qt][i], c * c4[i >> 2][i & 3]);
        const u32x4 u = {pack2(v[0], v[1]), pack2(v[2], v[3]), pack2(v[4], v[5]), pack2(v[6], v[7])};
        q0[qt] = __builtin_bit_cast(bf16x8, u);
        q1[qt] = (u32x4){h == 1 ? pack2(v[10], v[11]) : 0u, 0u, 0u, 0u};
      }
    }
    if (w >= 2) wait_flags(ws + WS_KFLG + (id & 7) * 1024 + (w - 2) * 512, 64, tag, 6);
    syncthreads();
  }
  __builtin_amdgcn_s_setprio(1);
  f32x16 o[2];
  const char* kvimg = ws + WS_KV;
  attn_loop_fast(smem, kvimg, w, l, q0, q1, o);
  bool bad = false;
#pragma unroll
  for (int qt = 0; qt < 2; ++qt) {
    bad |= !(o[qt][10] <= 1.8446744e19f);
#pragma unroll
    for (int i = 0; i < 10; ++i) bad |= !(__builtin_fabsf(o[qt][i]) <= 3.0e38f);
  }
  if (bad) *flag = 1u;
  syncthreads();
  if (__builtin_amdgcn_readfirstlane(*flag) != 0u) {
    q1[0][1] = 0u; q1[1][1] = 0u;
    attn_loop_safe(smem, kvimg, w, l, q0, q1, o);
  }
  const auto rs = __builtin_amdgcn_make_buffer_rsrc(ws + WS_OF, 0, 64 * 36 * 1024, 0x00020000);
#pragma unroll
  for (int qt = 0; qt < 2; ++qt) {
    const float inv = __builtin_amdgcn_rcpf(o[qt][10]);
    const unsigned dst = (tt0 + qt) * 36 * 1024;
    u32x4 u = {pack2(o[qt][0] * inv, o[qt][1] * inv), pack2(o[qt][2] * inv, o[qt][3] * inv),
               pack2(o[qt][4] * inv, o[qt][5] * inv), pack2(o[qt][6] * inv, o[qt][7] * inv)};
    __builtin_amdgcn_raw_buffer_store_b128(u, rs, dst + head * 1024 + l * 16, 0, 16);
    if (h == 0)
      __builtin_amdgcn_raw_buffer_store_b32(pack2(o[qt][8] * inv, o[qt][9] * inv), rs,
          dst + (32 + (head >> 3)) * 1024 + (((head >> 2) & 1) * 32 + r) * 16 + (head & 3) * 4, 0, 16);
  }
  WAIT_VM0();
  if (l == 0) set_flag(ws + WS_OFLG + tb * 256 + head * 8, tag);
}

DI void kv_stage(char* smem, const u16* __restrict__ x, const u16* __restrict__ wkv, int tt, int w, int l) {
  const int v = w - 1;
  const unsigned wo_ = (l >> 2) * 36 + ((l & 3) == 2 ? 20 : (l & 1) * 16);
  const char* wsrc = (const char*)wkv + v * 192 * 36;
  const unsigned lb = lds_addr(smem);
#pragma unroll 1
  for (int i = 0; i < 12; ++i) {
    glds16s(wo_, wsrc + i * 16 * 36, lb + 36864 + (v * 12 + i) * 1024);
    const int row = (i & 3) * 8 + (l >> 3);
    glds16s(row * 1152 + (((l & 7) ^ ((row >> 1) & 7)) << 4), (const char*)x + (size_t)tt * 32 * 1152 + (3 * v + (i >> 2)) * 128,
            lb + (v * 12 + i) * 1024);
  }
}
DI void kv_compute(char* smem, int mat, int w, int l, f32x16& acc, float& sx, float& sxx, float& cs) {
  const int r = l & 31, h = l >> 5, v = w - 1;
  const int g = l >> 4, q = (l & 15) >> 2, p = l & 3;
  const char* wb = smem + 36864 + v * 12288 + (8 * (g >> 1) + q) * 64 + (g & 1) * 32 + p * 8;
  auto kloop = [&](auto matc) {
    constexpr int MAT = decltype(matc)::value;
#pragma unroll 1
    for (int c = 0; c < 3; ++c) {
      bf16x8 wf[4], xf[4];
#pragma unroll
      for (int ks = 0; ks < 4; ++ks) {
        wf[ks] = tr_frag(wb + (c * 4 + ks) * 1024, wb + (c * 4 + ks) * 1024 + 256);
        xf[ks] = lds_frag(smem + (3 * v + c) * 4096 + r * 128 + (((2 * ks + h) ^ ((r >> 1) & 7)) << 4));
      }
#pragma unroll
      for (int ks = 0; ks < 4; ++ks) { if (MAT == 0) acc = MFMA32(wf[ks], xf[ks], acc); else acc = MFMA32(xf[ks], wf[ks], acc); }
#pragma unroll
      for (int ks = 0; ks < 4; ++ks) {
        const u32x4 ux = __builtin_bit_cast(u32x4, xf[ks]), uw = __builtin_bit_cast(u32x4, wf[ks]);
#pragma unroll
        for (int e = 0; e < 4; ++e) { sx = dot2(ux[e], ONES2, sx); sxx = dot2(ux[e], ux[e], sxx); cs = dot2(uw[e], ONES2, cs); }
      }
      asm volatile("" : "+v"(sx), "+v"(sxx), "+v"(cs));
    }
  };
  if (mat == 0) kloop(IC(0)); else kloop(IC(1));
}
DI void kv_publish(char* smem, int mat, int tt, int l, f32x16& ka, float sx, float sxx, float cs, char* __restrict__ ws, u64 tag) {
  const int r = l & 31, h = l >> 5;
  const char* red = smem + 55296 + l * 16;
#pragma unroll
  for (int s = 0; s < 2; ++s) {
#pragma unroll
    for (int gq = 0; gq < 4; ++gq) {
      const f32x4 v = *(const f32x4*)(red + s * 5120 + gq * 1024);
#pragma unroll
      for (int e = 0; e < 4; ++e) ka[4 * gq + e] += v[e];
    }
    const f32x4 v = *(const f32x4*)(red + s * 5120 + 4096);
    sx += v[0]; sxx += v[1]; cs += v[2];
  }
  sx = xhalf_sum(sx); sxx = xhalf_sum(sxx); cs = xhalf_sum(cs);
  const float mean = sx * (1.0f / DM);
  const float var = __builtin_fmaxf(sxx * (1.0f / DM) - mean * mean, 0.0f);
  const float a1 = 1.0f / __builtin_sqrtf(var + LN_EPS), c1 = -mean * a1;
  float* scr = (float*)(smem + 55296 + 10240);
  const auto rk = __builtin_amdgcn_make_buffer_rsrc(ws + WS_KV, 0, 64 * 2560, 0x00020000);
  const unsigned img = tt * 2560;
  if (h == 0) { scr[r] = mat == 0 ? cs : a1; scr[32 + r] = c1; }
  __builtin_amdgcn_s_waitcnt(0xc07f);
  __builtin_amdgcn_wave_barrier();
  float v[16];
#pragma unroll
  for (int gg = 0; gg < 4; ++gg) {
    const f32x4 p4 = *(const f32x4*)&scr[8 * gg + 4 * h], c4 = *(const f32x4*)&scr[32 + 8 * gg + 4 * h];
#pragma unroll
    for (int e = 0; e < 4; ++e)
      v[4 * gg + e] = mat == 0 ? __builtin_fmaf(a1, ka[4 * gg + e], c1 * p4[e]) : __builtin_fmaf(p4[e], ka[4 * gg + e], c4[e] * cs);
  }
  if (mat == 0) {
    const u32x4 u = {pack2(v[0], v[1]), pack2(v[2], v[3]), pack2(v[4], v[5]), pack2(v[6], v[7])};
    __builtin_amdgcn_raw_buffer_store_b128(u, rk, img + l * 16, 0, 16);
    const u32x2 u2 = {pack2(v[10], v[11]), 0x00003f80u};
    if (h == 1) __builtin_amdgcn_raw_buffer_store_b64(u2, rk, img + 1024 + r * 8, 0, 16);
  } else {
    const int vr = r >= 22 ? r - 6 : r;
#pragma unroll
    for (int s = 0; s < 2; ++s) {
      u32x4 u = {pack2(v[8 * s], v[8 * s + 1]), pack2(v[8 * s + 2], v[8 * s + 3]),
                 pack2(v[8 * s + 4], v[8 * s + 5]), pack2(v[8 * s + 6], v[8 * s + 7])};
      if (r == 18) u = (u32x4){ONES2, ONES2, ONES2, ONES2};
      if (r == 19) u = (u32x4){0u, 0u, 0u, 0u};
      if (r < 16 || r == 18 || r == 19 || r == 22 || r == 23)
        __builtin_amdgcn_raw_buffer_store_b128(u, rk, img + 1280 + ((s * 2 + h) * 20 + vr) * 16, 0, 16);
    }
  }
  WAIT_VM0();
  if (l < 8) set_flag(ws + WS_KFLG + l * 1024 + (tt * 2 + mat) * 8, tag);
}

DI void mlp_wg(char* smem, int id, const u16* __restrict__ wk, const u16* __restrict__ wv,
               const u16* __restrict__ w1, const u16* __restrict__ w2,
               const u16* __restrict__ wo, const u16* __restrict__ x, const u16* __restrict__ b1,
               const u16* __restrict__ b2, char* __restrict__ ws, u16* __restrict__ out, u64 tag) {
  const int tid = TID, l = tid & 63;
  const int w = __builtin_amdgcn_readfirstlane(tid >> 6);
  const int xcd = id & 7, j = id >> 3, rh = xcd >> 2, sq = xcd & 3;
  const int rb = rh * 8 + (j & 7), cb = sq * 4 + (j >> 3), nh = cb & 1;
  const int ttile = rb * 4 + w;
  const int h = l >> 5;
  const bool five = w < 2;
  unsigned woff1[5], woff2[5];
#pragma unroll
  for (int jj = 0; jj < 5; ++jj) {
    const int idx = (w + 4 * jj < 17 ? w + 4 * jj : 17) * 64 + l;
    const unsigned prow = idx / 36, pcol = (idx - prow * 36) * 16;
    woff1[jj] = prow * 9216 + pcol; woff2[jj] = prow * 1152 + pcol;
  }
  const char* w1src = (const char*)w1 + cb * 576;
  const char* w2src = (const char*)w2 + nh * 576;
  const char* ysrc = ws + WS_YF + (size_t)ttile * 36 * 1024;
  const char* hsrc = ws + WS_HF + (size_t)ttile * 288 * 1024;
  const char* osrc = ws + WS_OF + (size_t)ttile * 36 * 1024;
  const int pg = cb >> 1;
  const unsigned loff = l * 16;
  const unsigned lbase = lds_addr(smem) + w * 1024;
  auto wpiece = [&](const char* g, unsigned dst, int jj) {
    if (jj < 4 || five) glds16s(woff1[jj], g, dst + jj * 4096);
  };
  auto bload = [&](const char* p, u32x4 (&d)[2]) {
    asm volatile("global_load_dwordx4 %0, %2, %3 sc1\n\tglobal_load_dwordx4 %1, %2, %3 offset:1024 sc1"
                 : "=&v"(d[0]), "=&v"(d[1]) : "v"(loff), "s"(p) : "memory");
  };
  const char* wp = w1src + (size_t)3 * 32 * 9216;
  const char* bp = ysrc + 2 * 2048;
  unsigned wstride = 32 * 9216, bstride = 2048;
  const int g = l >> 4, q = (l & 15) >> 2, p = l & 3;
  const int aoff = (4 * h + q) * 576 + (g & 1) * 32 + p * 8;
  f32x16 acc[9];
  bf16x8 fa[5], fb[4];
  u32x4 br[3][2];
  auto rd = [&](const char* sb, int m) { return tr_frag(sb + m * 64, sb + m * 64 + 8 * 576); };
  auto touch_a = [&]() { asm volatile("" : "+v"(fa[0]), "+v"(fa[1]), "+v"(fa[2]), "+v"(fa[3]), "+v"(fa[4])); };
  auto touch_b = [&]() { asm volatile("" : "+v"(fb[0]), "+v"(fb[1]), "+v"(fb[2]), "+v"(fb[3])); };
  auto group_a = [&](const u32x4& b, const char* sb, auto job0, auto job1) {
    const bf16x8 bb = __builtin_bit_cast(bf16x8, b);
    touch_a();
    SCHED_FENCE();
    acc[0] = MFMA32(fa[0], bb, acc[0]); SCHED_FENCE();
    fb[0] = rd(sb, 5); fb[1] = rd(sb, 6); SCHED_FENCE();
    acc[1] = MFMA32(fa[1], bb, acc[1]); SCHED_FENCE();
    fb[2] = rd(sb, 7); fb[3] = rd(sb, 8); SCHED_FENCE();
    acc[2] = MFMA32(fa[2], bb, acc[2]); SCHED_FENCE();
    job0(); SCHED_FENCE();
    acc[3] = MFMA32(fa[3], bb, acc[3]); SCHED_FENCE();
    job1(); SCHED_FENCE();
    acc[4] = MFMA32(fa[4], bb, acc[4]); SCHED_FENCE();
  };
  auto group_b = [&](const u32x4& b, const char* sb, auto job0, auto job1) {
    const bf16x8 bb = __builtin_bit_cast(bf16x8, b);
    SCHED_FENCE();
    acc[5] = MFMA32(fb[0], bb, acc[5]); SCHED_FENCE();
    fa[0] = rd(sb, 0); fa[1] = rd(sb, 1); fa[2] = rd(sb, 2); SCHED_FENCE();
    acc[6] = MFMA32(fb[1], bb, acc[6]); SCHED_FENCE();
    fa[3] = rd(sb, 3); fa[4] = rd(sb, 4); SCHED_FENCE();
    acc[7] = MFMA32(fb[2], bb, acc[7]); SCHED_FENCE();
    job0(); SCHED_FENCE();
    acc[8] = MFMA32(fb[3], bb, acc[8]); SCHED_FENCE();
    job1(); SCHED_FENCE();
  };
  auto nojob = []() {};
  auto step = [&](int ph, int i, auto cc) {
    constexpr int C = decltype(cc)::value;
    const int n = ph * 18 + i;
    if (C == 0 && n == 12) __builtin_amdgcn_s_setprio(0);
    if (C == 2 && n == 20) {
      if (l == 0) set_flag(ws + WS_FLG + ((rb * 16 + cb) * 4 + w) * 64, tag);
    }
    if (C == 1 && n == 25) wait_flags(ws + WS_FLG + ((rb * 16 + (cb ^ 1)) * 4 + w) * 64, 1, tag, 0);
    if (C == 1 && n == 34) wait_flags(ws + WS_OFLG + (ttile >> 1) * 256 + (pg & 6) * 32, 8, tag, 5);
    const unsigned wd = lbase + ((n + 3) & 3) * 18432;
    const char* sb = smem + (n & 3) * 18432 + aoff;
    const char* sn = smem + ((n + 1) & 3) * 18432 + aoff;
    group_a(br[C][0], sb, [&]() { bload(bp, br[(C + 2) % 3]); }, [&]() { wpiece(wp, wd, 0); });
    touch_b();
    group_b(br[C][0], sb + 16 * 576, [&]() { wpiece(wp, wd, 1); }, [&]() { wpiece(wp, wd, 2); });
    group_a(br[C][1], sb + 16 * 576, [&]() { wpiece(wp, wd, 3); }, [&]() { wpiece(wp, wd, 4); });
    touch_b();
    if (five) WAIT_VM(12); else WAIT_VM(10);
    asm volatile("" : "+v"(br[(C + 1) % 3][0]), "+v"(br[(C + 1) % 3][1]));
    if (C == 1 && n == 37) {
      const u32x4 z = {0u, 0u, 0u, 0u};
      const u32x4 t = (pg >> 1) == 3 ? br[2][1] : br[2][0];
      br[2][0] = (pg & 1) ? z : t;
      br[2][1] = z;
    }
    BARRIER();
    group_b(br[C][1], sn, [&]() {
      wp += wstride;
      if (C == 2 && n == 14) {
        wp = w2src + (size_t)cb * 288 * 1152; wstride = 32 * 1152;
#pragma unroll
        for (int jj = 0; jj < 5; ++jj) woff1[jj] = woff2[jj];
      }
      if (C == 2 && n == 23) wp = w2src + (size_t)(cb ^ 1) * 288 * 1152;
      if (C == 2 && n == 32) {
        wp = (const char*)wo + nh * 576 + (size_t)pg * 72 * 1152; wstride = 36 * 1152;
#pragma unroll
        for (int jj = 0; jj < 5; ++jj) { const unsigned pr = woff2[jj] / 1152u; woff1[jj] = woff2[jj] + (pr >> 4) * 2 * 1152; }
      }
      if (C == 1 && n == 34) {
        wp = (const char*)wo + nh * 576 + (size_t)(pg >> 1) * 144 * 1152; wstride = 0;
#pragma unroll
        for (int jj = 0; jj < 5; ++jj) {
          const unsigned pr = woff2[jj] / 1152u, ii = pr & 15, j2 = 4 * (ii >> 3) + (ii & 3);
          woff1[jj] = woff2[jj] - pr * 1152u + (18 * (4 * ((ii >> 2) & 1) + (j2 >> 1)) + 16 + (j2 & 1)) * 1152u;
        }
      }
      if (C == 0 && n == 15) bstride = 0;
      bp += bstride;
      if (C == 0 && n == 24) bp = hsrc + (size_t)(cb ^ 1) * 18 * 1024;
      if (C == 0 && n == 33) bp = osrc + pg * 4 * 1024;
      if (C == 2 && n == 35) { bp = osrc + (32 + (pg >> 1)) * 1024 - ((pg >> 1) == 3 ? 1024 : 0); bstride = 0; }
    }, nojob);
  };
  __builtin_amdgcn_s_setprio(2);
  if (w == 1 && l < 36) glds16s(loff, (const char*)b1 + cb * 576, lds_addr(smem) + 73728);
  const bool kvd = cb < 8;
  if (w == 0) {
    ln8(x, rb * 128 + cb * 8, ws);
    WAIT_VM0();
    if (l == 0) set_flag(ws + WS_YFLG + (rb * 4 + (cb >> 2)) * 128 + (cb & 3) * 8, tag);
  } else if (kvd) {
    kv_stage(smem, x, (cb & 1) ? wv : wk, rb * 4 + (cb >> 1), w, l);
    WAIT_VM0();
    float sx = 0.f, sxx = 0.f, cs = 0.f;
#pragma unroll
    for (int i = 0; i < 16; ++i) acc[0][i] = 0.f;
    kv_compute(smem, cb & 1, w, l, acc[0], sx, sxx, cs);
    br[0][0] = __builtin_bit_cast(u32x4, (f32x4){sx, sxx, cs, 0.f});
  }
  if (kvd) {
    syncthreads();
    if (w >= 2) {
      char* red = smem + 55296 + (w - 2) * 5120 + l * 16;
#pragma unroll
      for (int gq = 0; gq < 4; ++gq)
        *(f32x4*)(red + gq * 1024) = (f32x4){acc[0][4 * gq], acc[0][4 * gq + 1], acc[0][4 * gq + 2], acc[0][4 * gq + 3]};
      *(u32x4*)(red + 4096) = br[0][0];
    }
    syncthreads();
    if (w == 1) {
      const f32x4 st = __builtin_bit_cast(f32x4, br[0][0]);
      kv_publish(smem, cb & 1, rb * 4 + (cb >> 1), l, acc[0], st[0], st[1], st[2], ws, tag);
    }
  }
#pragma unroll
  for (int st = 0; st < 3; ++st)
#pragma unroll
    for (int jj = 0; jj < 5; ++jj) wpiece(w1src + (size_t)st * 32 * 9216, lbase + st * 18432, jj);
  if (w != 0 && !kvd) __builtin_amdgcn_s_sleep(40);
  wait_flags(ws + WS_YFLG + ttile * 128, 4, tag, 6);
  bload(ysrc, br[0]); bload(ysrc + 2048, br[1]);
  WAIT_VM(4);
  BARRIER();
#pragma unroll
  for (int m = 0; m < 9; ++m)
#pragma unroll
    for (int gq = 0; gq < 4; ++gq) {
      const u32x2 bv = *(const u32x2*)(smem + 73728 + (m * 32 + 8 * gq + 4 * h) * 2);
      acc[m][4 * gq] = bflo(bv[0]); acc[m][4 * gq + 1] = bfhi(bv[0]);
      acc[m][4 * gq + 2] = bflo(bv[1]); acc[m][4 * gq + 3] = bfhi(bv[1]);
    }
  WAIT_VM0();
  const auto rp = __builtin_amdgcn_make_buffer_rsrc(ws + WS_PART, 0, 64 * 18 * 16 * 1024, 0x00020000);
  int nph = 2;
  asm volatile("" : "+s"(nph));
#pragma unroll 1
  for (int ph = 0; ph < nph; ++ph) {
    asm volatile("" : "+v"(br[0][0]), "+v"(br[0][1]));
    {
      const char* s0 = smem + ph * 2 * 18432 + aoff;
#pragma unroll
      for (int m = 0; m < 5; ++m) fa[m] = rd(s0, m);
    }
#pragma unroll 1
    for (int i = 0; i < 18 + 3 * ph; i += 3) { step(ph, i, IC(0)); step(ph, i + 1, IC(1)); step(ph, i + 2, IC(2)); }
    if (ph == 0) {
      const float k0 = -2.0f * 0.7978845608028654f * 1.4426950408889634f, k1 = k0 * 0.044715f;
      const auto rs = __builtin_amdgcn_make_buffer_rsrc(ws + WS_HF, 0, 64 * 288 * 1024, 0x00020000);
      const unsigned hbase = (ttile * 288 + cb * 18) * 1024 + l * 16;
      u32x4 hk[2][2];
      __builtin_amdgcn_s_setprio(2);
#pragma unroll
      for (int m = 0; m < 9; ++m) {
#pragma unroll
        for (int s = 0; s < 2; ++s) {
          u32x4 u;
#pragma unroll
          for (int e = 0; e < 4; ++e)
            u[e] = pack2(gelu_tanh(acc[m][8 * s + 2 * e], k0, k1), gelu_tanh(acc[m][8 * s + 2 * e + 1], k0, k1));
          __builtin_amdgcn_raw_buffer_store_b128(u, rs, hbase + (m * 2 + s) * 1024, 0, 16);
          if (m < 2) hk[m][s] = u;
        }
      }
      WAIT_VM(18);
      asm volatile("" :: "v"(br[0][0]), "v"(br[0][1]), "v"(br[1][0]), "v"(br[1][1]));
      __builtin_amdgcn_s_setprio(0);
      br[0][0] = hk[0][0]; br[0][1] = hk[0][1]; br[1][0] = hk[1][0]; br[1][1] = hk[1][1];
      bp = hsrc + (size_t)cb * 18 * 1024 + 4096; bstride = 2048;
#pragma unroll
      for (int m = 0; m < 9; ++m)
#pragma unroll
        for (int i = 0; i < 16; ++i) acc[m][i] = 0.f;
    }
  }
#pragma unroll
  for (int m = 0; m < 9; ++m)
#pragma unroll
    for (int s = 0; s < 2; ++s) {
      const u32x4 u = {pack2(acc[m][8 * s], acc[m][8 * s + 1]), pack2(acc[m][8 * s + 2], acc[m][8 * s + 3]),
                       pack2(acc[m][8 * s + 4], acc[m][8 * s + 5]), pack2(acc[m][8 * s + 6], acc[m][8 * s + 7])};
      __builtin_amdgcn_raw_buffer_store_b128(u, rp, (((ttile * 18 + nh * 9 + m) * 2 + s) * 8 + pg) * 1024 + l * 16, 0, 16);
    }
  const int ft = rb * 4 + (pg >> 1), fs = pg & 1;
  const int nm = w == 0 ? 3 : 2, m0 = w == 0 ? 0 : 2 * w + 1;
  const size_t obase = (size_t)(ft * 32 + (l & 31)) * DM + nh * 288 + m0 * 32 + 16 * fs + 4 * h;
  u32x2 xv[3][2], bv[3][2];
#pragma unroll
  for (int m = 0; m < 3; ++m)
#pragma unroll
    for (int g2 = 0; g2 < 2; ++g2) {
      const int mm = m < nm ? m : 0;
      xv[m][g2] = *(const u32x2*)(x + obase + mm * 32 + 8 * g2);
      bv[m][g2] = *(const u32x2*)(b2 + nh * 288 + (m0 + mm) * 32 + 16 * fs + 4 * h + 8 * g2);
    }
  WAIT_VM0();
  syncthreads();
  if (tid == 0) set_flag(ws + WS_PFLG + (rb * 2 + nh) * 128 + pg * 8, tag);
  asm volatile("" :: "v"(br[0][0]), "v"(br[0][1]), "v"(br[1][0]), "v"(br[1][1]));
  u32x4 pv[3][8];
#pragma unroll
  for (int hs = 0; hs < 2; ++hs) {
    const int s0 = ((pg >> 2) ^ hs) * 4;
    wait_flags(ws + WS_PFLG + (rb * 2 + nh) * 128 + s0 * 8, 4, tag, 0);
#pragma unroll
    for (int m = 0; m < 3; ++m)
#pragma unroll
      for (int i = 0; i < 4; ++i)
        pv[m][hs * 4 + i] = __builtin_amdgcn_raw_buffer_load_b128(
            rp, (((ft * 18 + nh * 9 + m0 + (m < nm ? m : 0)) * 2 + fs) * 8 + s0 + i) * 1024 + l * 16, 0, 16);
  }
#pragma unroll
  for (int m = 0; m < 3; ++m) {
    float v[8];
#pragma unroll
    for (int g2 = 0; g2 < 2; ++g2) {
      v[4 * g2 + 0] = bflo(xv[m][g2][0]) + bflo(bv[m][g2][0]);
      v[4 * g2 + 1] = bfhi(xv[m][g2][0]) + bfhi(bv[m][g2][0]);
      v[4 * g2 + 2] = bflo(xv[m][g2][1]) + bflo(bv[m][g2][1]);
      v[4 * g2 + 3] = bfhi(xv[m][g2][1]) + bfhi(bv[m][g2][1]);
    }
#pragma unroll
    for (int src = 0; src < 8; ++src)
#pragma unroll
      for (int e = 0; e < 4; ++e) { const unsigned u = pv[m][src][e]; v[2 * e] += bflo(u); v[2 * e + 1] += bfhi(u); }
    if (m < nm) {
#pragma unroll
      for (int g2 = 0; g2 < 2; ++g2)
        *(u32x2*)(out + obase + m * 32 + 8 * g2) = (u32x2){pack2(v[4 * g2], v[4 * g2 + 1]), pack2(v[4 * g2 + 2], v[4 * g2 + 3])};
    }
  }
}

__global__ __launch_bounds__(256, 2) void fused_kernel(const u16* __restrict__ x, const u16* __restrict__ wq,
                                                       const u16* __restrict__ wk, const u16* __restrict__ wv,
                                                       const u16* __restrict__ wo, const u16* __restrict__ w1,
                                                       const u16* __restrict__ b1, const u16* __restrict__ w2,
                                                       const u16* __restrict__ b2, char* __restrict__ ws,
                                                       u16* __restrict__ out) {
  extern __shared__ __attribute__((aligned(16))) char smem[];
  const int b = BID;
  const u64 ev = __hip_atomic_load((GAS u64*)(ws + WS_EPOCH), __ATOMIC_RELAXED, __HIP_MEMORY_SCOPE_AGENT);
  const u64 epoch = ((u64)(unsigned)__builtin_amdgcn_readfirstlane((int)(ev >> 32)) << 32) |
                    (unsigned)__builtin_amdgcn_readfirstlane((int)ev);
  u64 tag = (epoch ^ 0x9E3779B97F4A7C15ull) * 0xBF58476D1CE4E5B9ull;
  tag ^= tag >> 29;
  syncthreads();
  if (TID == 0) set_flag(ws + WS_RFLG + b * 8, tag);
  if (b < 256) {
    const char* pc = (const char*)__builtin_amdgcn_s_getpc();
    unsigned d0, d1;
    const unsigned off = TID * 64;
    asm volatile("global_load_dword %0, %2, %3\n\tglobal_load_dword %1, %4, %3"
                 : "=&v"(d0), "=&v"(d1) : "v"(off), "s"(pc), "v"(off < K1_CODE - 16384 ? off + 16384 : off) : "memory");
    attn_wg(smem, b, x, wq, ws, tag);
    asm volatile("" :: "v"(d0), "v"(d1));
    if (b == 0 && TID < 64) {
#pragma unroll 1
      for (int i = 0; i < 8; ++i) wait_flags(ws + WS_RFLG + i * 512, 64, tag, 0);
      if (TID == 0) __hip_atomic_store((GAS u64*)(ws + WS_EPOCH), epoch + 1, __ATOMIC_RELAXED, __HIP_MEMORY_SCOPE_AGENT);
    }
  } else {
    mlp_wg(smem, b - 256, wk, wv, w1, w2, wo, x, b1, b2, ws, out, tag);
  }
}

#if defined(__HIP_DEVICE_COMPILE__)
#pragma clang attribute pop
#endif

extern "C" void kernel_launch(void* const* d_in, const int* in_sizes, int n_in, void* d_out, int out_size,
                              void* d_ws, size_t ws_size, hipStream_t stream) {
  const u16* x = (const u16*)d_in[0];
  const u16* wq = (const u16*)d_in[1];
  const u16* wk = (const u16*)d_in[2];
  const u16* wv = (const u16*)d_in[3];
  const u16* wo = (const u16*)d_in[4];
  const u16* w1 = (const u16*)d_in[6];
  const u16* b1 = (const u16*)d_in[7];
  const u16* w2 = (const u16*)d_in[8];
  const u16* b2 = (const u16*)d_in[9];
  char* ws = (char*)d_ws;
  (void)hipFuncSetAttribute((const void*)fused_kernel, hipFuncAttributeMaxDynamicSharedMemorySize, K1_LDS);
  fused_kernel<<<512, 256, K1_LDS, stream>>>(x, wq, wk, wv, wo, w1, b1, w2, b2, ws, (u16*)d_out);
}
```
